```python
import torch

def _softmax(x, axis=-1):
    return torch.softmax(x, dim=axis)

def _gelu(x):
    return torch.nn.functional.gelu(x, approximate='tanh')

def _silu(x):
    return torch.nn.functional.silu(x)

def _sigmoid(x):
    return torch.sigmoid(x)

def _layernorm(x, axis=-1, eps=1e-05):
    m = x.mean(dim=axis, keepdim=True)
    v = x.var(dim=axis, keepdim=True, unbiased=False)
    return (x - m) / torch.sqrt(v + eps)

def _scatter_add(tgt, idx, src, axis=0):
    return tgt.clone().index_add_(axis, idx, src)

def _segment_sum(src, seg, n):
    out = torch.zeros((n,) + src.shape[1:], dtype=src.dtype)
    return out.index_add_(0, seg, src)

def _pad(x, pw):
    flat = tuple((v for pair in reversed(pw) for v in pair))
    return torch.nn.functional.pad(x, flat)
kernel_name = 'gated_mix__memory_aug_recurrent_mem_tr__moe_arch_mod__prompt'

def setup_inputs():

    _w = lambda a: (lambda t: t.to(torch.bfloat16) if t.is_floating_point() else t)(torch.as_tensor(a))

    def _A_setup():
        B, S, Dm = (1, 2048, 512)
        H, Dh = (32, 128)
        M, seg = (8, 32)
        seg = min(seg, S)
        S = S // seg * seg
        return {'x': _w((lambda *s: torch.randn(*s, dtype=torch.bfloat16) * 0.05)(B, S, Dm)), 'mem0': _w((lambda *s: torch.randn(*s, dtype=torch.bfloat16) * 0.8)(B, M, Dm)), 'Wq': _w((lambda *s: torch.randn(*s, dtype=torch.bfloat16) * 3.2)(Dm, H, Dh) * Dm ** (-0.5)), 'Wk': _w((lambda *s: torch.randn(*s, dtype=torch.bfloat16) * 3.2)(Dm, H, Dh) * Dm ** (-0.5)), 'Wv': _w((lambda *s: torch.randn(*s, dtype=torch.bfloat16) * 0.8)(Dm, H, Dh) * Dm ** (-0.5)), 'Wo': _w((lambda *s: torch.randn(*s, dtype=torch.bfloat16) * 0.8)(H, Dh, Dm) * (H * Dh) ** (-0.5)), 'seg_len': seg}
    _A_in = _A_setup()

    def _B_setup():
        B, S, D, F, C = (1, 2048, 512, 1792, 8)
        return {'x': _w((lambda *s: torch.randn(*s, dtype=torch.bfloat16) * 0.05)(B, S, D)), 'w_r': _w((lambda *s: torch.randn(*s, dtype=torch.bfloat16) * 1.6)(D)), 'w_gate': _w((lambda *s: torch.randn(*s, dtype=torch.bfloat16) * 0.2)(D, F)), 'w_up': _w((lambda *s: torch.randn(*s, dtype=torch.bfloat16) * 0.2)(D, F)), 'w_down': _w((lambda *s: torch.randn(*s, dtype=torch.bfloat16) * 0.2)(F, D))}
    _B_in = _B_setup()
    return {'A_x': _A_in['x'], 'A_mem0': _A_in['mem0'], 'A_Wq': _A_in['Wq'], 'A_Wk': _A_in['Wk'], 'A_Wv': _A_in['Wv'], 'A_Wo': _A_in['Wo'], 'A_seg_len': _A_in['seg_len'], 'B_x': _B_in['x'], 'B_w_r': _B_in['w_r'], 'B_w_gate': _B_in['w_gate'], 'B_w_up': _B_in['w_up'], 'B_w_down': _B_in['w_down'], 'gate_w': _w((lambda *s: torch.randn(*s, dtype=torch.bfloat16) * 0.05)(512) * 16.0)}

def reference(A_x, A_mem0, A_Wq, A_Wk, A_Wv, A_Wo, A_seg_len, B_x, B_w_r, B_w_gate, B_w_up, B_w_down, gate_w):

    def _A_ref(x, mem0, Wq, Wk, Wv, Wo, seg_len):
        B, S, Dm = x.shape
        H, Dh = (Wq.shape[1], Wq.shape[2])
        M = mem0.shape[1]
        scale = Dh ** (-0.5)
        n_seg = S // seg_len
        L = M + seg_len
        idx = torch.arange(L)
        mask = (idx[:, None] < M) | (idx[:, None] >= idx[None, :])
        mem = mem0
        outs = []
        for s in range(n_seg):
            xs = x[:, s * seg_len:(s + 1) * seg_len, :].clone()
            h = torch.concatenate([mem, xs], axis=1)
            q = torch.einsum('btd,dhe->bhte', h, Wq)
            k = torch.einsum('btd,dhe->bhte', h, Wk)
            v = torch.einsum('btd,dhe->bhte', h, Wv)
            scores = torch.einsum('bhqe,bhke->bhqk', q, k) * scale
            scores = torch.where(mask[None, None], scores, -1000000000.0)
            p = _softmax(scores, axis=-1)
            o = torch.einsum('bhqk,bhke->bhqe', p, v)
            o = torch.einsum('bhte,hed->btd', o, Wo)
            mem = o[:, :M, :].clone()
            outs.append(o[:, M:, :])
        return torch.concatenate(outs, axis=1)

    def _B_ref(x, w_r, w_gate, w_up, w_down):
        B, S, D = x.shape
        C = 8
        r = x @ w_r
        idx = torch.argsort(-r, axis=-1)[:, :C]
        idx_e = (lambda x, n, axis: torch.repeat_interleave(x, n, dim=axis))(idx.reshape(B, C, 1), D, axis=2)
        x_sel = (lambda x, i, axis: torch.take_along_dim(x, i, dim=axis))(x, idx_e, axis=1)
        g = _silu(x_sel @ w_gate)
        u = x_sel @ w_up
        y = g * u @ w_down
        r_sel = _sigmoid((lambda x, i, axis: torch.take_along_dim(x, i, dim=axis))(r, idx, axis=1))
        y = y * r_sel.reshape(B, C, 1)
        b_off = (torch.arange(B) * S).reshape(B, 1)
        flat_idx = (idx + b_off).reshape(B * C)
        zero = (x * 0.0).reshape(B * S, D)
        upd = _scatter_add(zero, flat_idx, y.reshape(B * C, D), axis=0)
        return x + upd.reshape(B, S, D)
    x = A_x
    g = _sigmoid((x * gate_w).sum(-1, keepdims=True))
    a = _A_ref(x, A_mem0, A_Wq, A_Wk, A_Wv, A_Wo, A_seg_len)
    b = _B_ref(x, B_w_r, B_w_gate, B_w_up, B_w_down)
    return g * a + (1 - g) * b
```

```cpp
#include <hip/hip_runtime.h>
#include <hip/hip_bf16.h>
#include <stdint.h>

typedef __bf16 bf16x2 __attribute__((ext_vector_type(2)));
typedef __bf16 bf16x8 __attribute__((ext_vector_type(8)));
typedef __bf16 bf16x16 __attribute__((ext_vector_type(16)));
typedef short s16x4 __attribute__((ext_vector_type(4)));
typedef float f32x2 __attribute__((ext_vector_type(2)));
typedef float f32x4 __attribute__((ext_vector_type(4)));
typedef unsigned u32x2 __attribute__((ext_vector_type(2)));
typedef unsigned u32x4 __attribute__((ext_vector_type(4)));
typedef unsigned u32x8 __attribute__((ext_vector_type(8)));
typedef __amdgpu_buffer_rsrc_t rsrc_t;
#define DI __device__ __forceinline__
#define LAS __attribute__((address_space(3)))
#define GAS __attribute__((address_space(1)))
typedef LAS unsigned char* ldsp;

constexpr int SEQ = 2048, DM = 512, NH = 32, NSEG = 64, FF = 1792;
constexpr unsigned SENT = 0xFFFFFFFFu;
constexpr int SC1 = 16;
constexpr unsigned SPIN_CAP = 1u << 17;

constexpr int FL_ABORT = 0, FL_POL = 128, FL_INIT = 256, FL_RDONE = 1024, FL_HDONE = 1536, FL_KV = 2048, FL_MD = FL_KV + 64 * 128, FL_OD = FL_MD + 64 * 128;
constexpr int FL_BYTES = FL_OD + 64 * 128;
constexpr unsigned MB = 1u << 20;
constexpr unsigned WS_PART = 0;
constexpr unsigned WS_MEM = 2 * MB;
constexpr unsigned WS_TIME = WS_MEM + 262144;
constexpr unsigned WS_R = 3 * MB, WS_G = WS_R + 8192, WS_HB = WS_G + 8192;
constexpr unsigned WS_KX = 4 * MB, WS_VX = 36 * MB, WS_OT = 68 * MB, WS_KM = 100 * MB, WS_VM = WS_KM + 4096, WS_DBG = 132 * MB, WS_END = 136 * MB;
constexpr int LDS_BYTES = 160 * 1024;
#define STAMPG(i) do {} while (0)
#define STAMP(i) do {} while (0)
#define STAMP2(i) do {} while (0)
#define DBGV(i, v) do {} while (0)
#define PT(n) do {} while (0)
#define RT() 0u
#define CT() 0u
#define DBG_DUMP() do {} while (0)
constexpr int L_MISC = 128 * 1024;

constexpr int CODE_BYTES = 54 * 1024;
struct Params {
  const uint16_t *x, *mem0, *wq, *wk, *wv, *wo, *wr, *wg, *wu, *wd, *gw;
  uint16_t* out;
  unsigned char* flags;
  unsigned char* data;
  int d1, d2, pen, t0, tdel, zb0, cap, pad1;
};

DI unsigned rfl(unsigned v) { return __builtin_amdgcn_readfirstlane(v); }
DI unsigned cvtpk(float lo, float hi) { f32x2 v = {lo, hi}; return __builtin_bit_cast(unsigned, __builtin_convertvector(v, bf16x2)); }
DI float bflo(unsigned u) { return __builtin_bit_cast(float, u << 16); }
DI float bfhi(unsigned u) { return __builtin_bit_cast(float, u & 0xffff0000u); }
DI float rbf(float x) { return bflo(cvtpk(x, x)); }
DI unsigned f2u(float x) { return __builtin_bit_cast(unsigned, x); }
DI float u2f(unsigned x) { return __builtin_bit_cast(float, x); }
DI unsigned mtime() { unsigned long long t; asm volatile("s_memtime %0\n\ts_waitcnt lgkmcnt(0)" : "=s"(t) :: "memory"); return (unsigned)t; }
DI void wait_cyc(unsigned t0, int d) { while ((int)(mtime() - t0) < d) {} }
#define BAR() asm volatile("s_waitcnt lgkmcnt(0)\n\ts_barrier" ::: "memory")
#define VM0() asm volatile("s_waitcnt vmcnt(0)" ::: "memory")
#define PIN() __builtin_amdgcn_sched_barrier(0)
#define SM_A(c, a, b, ix) asm volatile("v_smfmac_f32_16x16x64_bf16 %0, %1, %2, %3" : "+v"(c) : "v"(a), "a"(b), "v"(ix) : "memory")
#define SM_V(c, a, b, ix) asm volatile("v_smfmac_f32_16x16x64_bf16 %0, %1, %2, %3" : "+v"(c) : "v"(a), "v"(b), "v"(ix) : "memory")
#define MF(c, a, b) asm volatile("v_mfma_f32_16x16x32_bf16 %0, %1, %2, %0" : "+v"(c) : "v"(a), "v"(b) : "memory")
#define MF0(c, a, b) asm volatile("v_mfma_f32_16x16x32_bf16 %0, %1, %2, 0" : "=&v"(c) : "v"(a), "v"(b) : "memory")
#define MF_AV(c, a, b) asm volatile("v_mfma_f32_16x16x32_bf16 %0, %1, %2, %0" : "+v"(c) : "a"(a), "v"(b) : "memory")
#define MF0_AV(c, a, b) asm volatile("v_mfma_f32_16x16x32_bf16 %0, %1, %2, 0" : "=&v"(c) : "a"(a), "v"(b) : "memory")
#define MF_VA(c, a, b) asm volatile("v_mfma_f32_16x16x32_bf16 %0, %1, %2, %0" : "+v"(c) : "v"(a), "a"(b) : "memory")
#define MF0_VA(c, a, b) asm volatile("v_mfma_f32_16x16x32_bf16 %0, %1, %2, 0" : "=&v"(c) : "v"(a), "a"(b) : "memory")
#define MFH(c, a, b) asm volatile("v_mfma_f32_16x16x16_bf16 %0, %1, %2, %0" : "+v"(c) : "v"(a), "v"(b) : "memory")
#define ZERO4(c) do { float z_ = 0.f; asm volatile("" : "+v"(z_)); c = (f32x4){z_, z_, z_, z_}; asm volatile("" : "+v"(c)); } while (0)
#define NOPS(n) asm volatile("s_nop %0" :: "n"(n) : "memory")
#define USE1(x) asm volatile("" : "+v"(x))
#define USE2(x, y) asm volatile("" : "+v"(x), "+v"(y))
#define USE4(x, y, z, t) asm volatile("" : "+v"(x), "+v"(y), "+v"(z), "+v"(t))
DI float fmx(float a, float b) { float r; asm("v_max_f32_e32 %0, %1, %2" : "=v"(r) : "v"(a), "v"(b)); return r; }
DI float fmx3(float a, float b, float c) { float r; asm("v_max3_f32 %0, %1, %2, %3" : "=v"(r) : "v"(a), "v"(b), "v"(c)); return r; }
DI bf16x8 asb8(u32x4 v) { return __builtin_bit_cast(bf16x8, v); }
DI bf16x8 lo8(bf16x16 v) { return __builtin_shufflevector(v, v, 0, 1, 2, 3, 4, 5, 6, 7); }
DI bf16x8 hi8(bf16x16 v) { return __builtin_shufflevector(v, v, 8, 9, 10, 11, 12, 13, 14, 15); }
DI u32x4 ld4(rsrc_t r, unsigned vo, unsigned so) { return __builtin_amdgcn_raw_buffer_load_b128(r, vo, so, SC1); }
DI unsigned ld1(rsrc_t r, unsigned vo, unsigned so) { return __builtin_amdgcn_raw_buffer_load_b32(r, vo, so, SC1); }
DI void st4(u32x4 v, rsrc_t r, unsigned vo, unsigned so) { __builtin_amdgcn_raw_buffer_store_b128(v, r, vo, so, SC1); }
DI void st1(unsigned v, rsrc_t r, unsigned vo, unsigned so) { __builtin_amdgcn_raw_buffer_store_b32(v, r, vo, so, SC1); }
DI unsigned max4(u32x4 v) { return max(max(v.x, v.y), max(v.z, v.w)); }
DI u32x4 lds4(ldsp p) { return *(LAS u32x4*)p; }
DI u32x2 trrd(ldsp p) { return __builtin_bit_cast(u32x2, __builtin_amdgcn_ds_read_tr16_b64_v4i16((LAS s16x4*)p)); }
DI float sum32(float a, float b) {
  auto r = __builtin_amdgcn_permlane32_swap(f2u(a), f2u(b), false, false); return u2f(r[0]) + u2f(r[1]); }
DI float sum16(float a, float b) {
  auto r = __builtin_amdgcn_permlane16_swap(f2u(a), f2u(b), false, false); return u2f(r[0]) + u2f(r[1]); }
DI float allmax_g(float v) {
  auto r = __builtin_amdgcn_permlane32_swap(f2u(v), f2u(v), false, false); v = fmx(u2f(r[0]), u2f(r[1]));
  auto q = __builtin_amdgcn_permlane16_swap(f2u(v), f2u(v), false, false); return fmx(u2f(q[0]), u2f(q[1])); }
DI float ror8(float v) { return u2f(__builtin_amdgcn_update_dpp(0, (int)f2u(v), 0x128, 0xf, 0xf, false)); }
DI float sel_c8(float a, float b) { return u2f(__builtin_amdgcn_update_dpp((int)f2u(a), (int)f2u(b), 0xE4, 0xf, 0xc, false)); }
DI float sel_l32(float a, float b) { return u2f(__builtin_amdgcn_update_dpp((int)f2u(a), (int)f2u(b), 0xE4, 0xc, 0xf, false)); }
DI unsigned lo_half(unsigned v) { return __builtin_amdgcn_update_dpp((int)v, (int)v, 0x128, 0xf, 0xc, false); }
DI unsigned hi_half(unsigned v) { return __builtin_amdgcn_update_dpp((int)v, (int)v, 0x128, 0xf, 0x3, false); }
DI float allsum_g(float v) { v = sum32(v, v); return sum16(v, v); }
DI float ex2(float x) { return __builtin_amdgcn_exp2f(x); }
constexpr float SCALE = 0.08838834764831845f, LOG2E = 1.4426950408889634f;
DI float rscore(float acc) { return rbf(rbf(acc) * SCALE); }
constexpr float SARG = SCALE * LOG2E;
DI f32x2 rscore2(float a, float b) { return (f32x2){a, b}; }
DI float rscore1(float a) { return a; }
DI unsigned blk(int seg, int h, unsigned far) { return (unsigned)(seg * NH + h) * 16384u + ((((unsigned)seg >> 2) ^ far) & 1u) * 8192u; }
constexpr unsigned OOB = 0x80000000u;

DI void wait_flag(rsrc_t fl, unsigned off, unsigned want, bool& dead) {
  for (unsigned sp = 0; !dead; ++sp) {
    asm volatile("" ::: "memory");
    if (rfl(ld1(fl, off, 0)) >= want) return;
    __builtin_amdgcn_s_sleep(4);
    if ((sp & 63u) == 63u && rfl(ld1(fl, FL_ABORT, 0))) dead = true;
    if (sp > SPIN_CAP) { st1(1u, fl, FL_ABORT, 0); dead = true; }
  }
}
DI unsigned wait_flags(rsrc_t fl, unsigned voff, unsigned want, bool& dead) {
  unsigned v = 0;
  for (unsigned sp = 0; !dead; ++sp) {
    asm volatile("" ::: "memory");
    v = ld1(fl, voff, 0);
    if (__all(v >= want)) break;
    __builtin_amdgcn_s_sleep(4);
    if ((sp & 63u) == 63u && rfl(ld1(fl, FL_ABORT, 0))) dead = true;
    if (sp > SPIN_CAP) { st1(1u, fl, FL_ABORT, 0); dead = true; }
  }
  return v;
}
DI bool spin_fail(rsrc_t fl, unsigned sp, bool& dead) {
  if ((sp & 255u) == 255u && rfl(ld1(fl, FL_ABORT, 0))) dead = true;
  if (sp > 8 * SPIN_CAP) { st1(1u, fl, FL_ABORT, 0); dead = true; }
  return dead;
}

DI void glds16(const void* gsrc, unsigned lds_dst) {
  unsigned keep;
  asm volatile("s_mov_b32 %0, m0\n\ts_mov_b32 m0, %2\n\ts_nop 0\n\tglobal_load_lds_dwordx4 %1, off\n\ts_mov_b32 m0, %0" : "=&s"(keep) : "v"(gsrc), "s"(lds_dst) : "memory");
}
DI void slot_issue(const unsigned char* lane_src, unsigned row_stride, unsigned lds_dst) {
#pragma unroll
  for (int i = 0; i < 4; ++i) glds16(lane_src + (size_t)i * 16 * row_stride, lds_dst + i * 1024);
}
template <int N> DI void vmwait() { asm volatile("s_waitcnt vmcnt(%0)" :: "n"(N) : "memory"); }
template <int O0, int O1, int O2, int O3> DI bf16x16 tr4(unsigned base) {
  u32x2 a, b, c, d;
  asm volatile("ds_read_b64_tr_b16 %0, %4 offset:%5\n\tds_read_b64_tr_b16 %1, %4 offset:%6\n\tds_read_b64_tr_b16 %2, %4 offset:%7\n\tds_read_b64_tr_b16 %3, %4 offset:%8\n\ts_waitcnt lgkmcnt(0)"
               : "=&v"(a), "=&v"(b), "=&v"(c), "=&v"(d) : "v"(base), "n"(O0), "n"(O1), "n"(O2), "n"(O3) : "memory");
  u32x8 r = {a.x, a.y, b.x, b.y, c.x, c.y, d.x, d.y};
  return __builtin_bit_cast(bf16x16, r);
}
template <int O0, int O1, int O2, int O3> DI void tr8(unsigned b0, unsigned b1, bf16x16& t0, bf16x16& t1) {
  u32x2 a, b, c, d, e, f, g, h;
  asm volatile("ds_read_b64_tr_b16 %0, %8 offset:%10\n\tds_read_b64_tr_b16 %1, %8 offset:%11\n\tds_read_b64_tr_b16 %2, %8 offset:%12\n\tds_read_b64_tr_b16 %3, %8 offset:%13\n\t"
               "ds_read_b64_tr_b16 %4, %9 offset:%10\n\tds_read_b64_tr_b16 %5, %9 offset:%11\n\tds_read_b64_tr_b16 %6, %9 offset:%12\n\tds_read_b64_tr_b16 %7, %9 offset:%13\n\ts_waitcnt lgkmcnt(0)"
               : "=&v"(a), "=&v"(b), "=&v"(c), "=&v"(d), "=&v"(e), "=&v"(f), "=&v"(g), "=&v"(h) : "v"(b0), "v"(b1), "n"(O0), "n"(O1), "n"(O2), "n"(O3) : "memory");
  u32x8 r = {a.x, a.y, b.x, b.y, c.x, c.y, d.x, d.y}, q = {e.x, e.y, f.x, f.y, g.x, g.y, h.x, h.y};
  t0 = __builtin_bit_cast(bf16x16, r); t1 = __builtin_bit_cast(bf16x16, q);
}
template <int O0, int O1, int O2, int O3> DI void tr8a(unsigned b0, unsigned b1, bf16x8& r0, bf16x8& r1, bf16x8& r2, bf16x8& r3) {
  u32x2 a, b, c, d, e, f, g, h;
  asm volatile("ds_read_b64_tr_b16 %0, %8 offset:%10\n\tds_read_b64_tr_b16 %1, %8 offset:%11\n\tds_read_b64_tr_b16 %2, %8 offset:%12\n\tds_read_b64_tr_b16 %3, %8 offset:%13\n\t"
               "ds_read_b64_tr_b16 %4, %9 offset:%10\n\tds_read_b64_tr_b16 %5, %9 offset:%11\n\tds_read_b64_tr_b16 %6, %9 offset:%12\n\tds_read_b64_tr_b16 %7, %9 offset:%13\n\ts_waitcnt lgkmcnt(0)"
               : "=&a"(a), "=&a"(b), "=&a"(c), "=&a"(d), "=&a"(e), "=&a"(f), "=&a"(g), "=&a"(h) : "v"(b0), "v"(b1), "n"(O0), "n"(O1), "n"(O2), "n"(O3) : "memory");
  r0 = asb8((u32x4){a.x, a.y, b.x, b.y}); r1 = asb8((u32x4){c.x, c.y, d.x, d.y}); r2 = asb8((u32x4){e.x, e.y, f.x, f.y}); r3 = asb8((u32x4){g.x, g.y, h.x, h.y});
  asm volatile("" : "+a"(r0), "+a"(r1), "+a"(r2), "+a"(r3));
}
template <int SLOT, int TILE> DI bf16x16 frag_nat(unsigned nb) { constexpr int B = (SLOT & 7) * 4096 + TILE * 32; return tr4<B, B + 4 * 64, B + 32 * 64, B + 36 * 64>(nb); }
template <int SLOT, int TILE> DI bf16x16 frag_pi(unsigned pb) { constexpr int B = (SLOT & 7) * 4096 + TILE * 32; return tr4<B, B + 16 * 64, B + 32 * 64, B + 48 * 64>(pb); }
#define TO_A(x) asm volatile("" : "+a"(x))
template <int I, int N, class FI, class FC> DI void load_stream(FI&& issue, FC&& consume) {
  if constexpr (I == 0) {
#pragma unroll
    for (int i = 0; i < (N < 8 ? N : 8); ++i) issue(i);
  }
  if constexpr (I < N) {
    constexpr int infl = (N - I) < 8 ? (N - I) : 8;
    vmwait<4 * (infl - 1)>();
    consume(std::integral_constant<int, I>{});
    if constexpr (I + 8 < N) issue(I + 8);
    load_stream<I + 1, N>(issue, consume);
  }
}

DI void glds16s(const void* sbase, unsigned voff, unsigned lds_dst) {
  unsigned keep;
  asm volatile("s_mov_b32 %0, m0\n\ts_mov_b32 m0, %3\n\ts_nop 0\n\tglobal_load_lds_dwordx4 %1, %2\n\ts_mov_b32 m0, %0" : "=&s"(keep) : "v"(voff), "s"(sbase), "s"(lds_dst) : "memory");
}
template <int I, int N, int NI, class FI, class FC> DI void load_blocks(FI&& issue, FC&& consume) {
  if constexpr (I == 0) {
#pragma unroll
    for (int i = 0; i < 7; ++i) issue(i);
  }
  if constexpr (I < N) {
    constexpr int infl = (NI - I) < 7 ? (NI - I) : 7;
    vmwait<4 * (infl - 1)>();
    asm volatile("s_barrier" ::: "memory");
    if constexpr (I + 7 < NI) issue(I + 7);
    consume(std::integral_constant<int, I>{});
    asm volatile("s_waitcnt lgkmcnt(0)" ::: "memory");
    load_blocks<I + 1, N, NI>(issue, consume);
  }
}
struct Rows256 {
  unsigned vo[2], rd[2];
  DI Rows256(int lane, int w, int h, unsigned ldsb) {
    const int g = lane >> 4, q = (lane >> 2) & 3, pp = lane & 3;
#pragma unroll
    for (int k = 0; k < 2; ++k) {
      vo[k] = (unsigned)((16 * w + g) * 8192 + h * 256 + (((lane & 15) ^ (g << 1) ^ (k << 3)) * 16));
      rd[k] = ldsb + (8 * g + q) * 256 + (((4 * w + 2 * k + (pp >> 1)) ^ (q << 1) ^ ((g & 1) << 3)) * 16) + (pp & 1) * 8;
    }
  }
  DI void issue(const uint16_t* m, int ks, unsigned dst) const {
#pragma unroll
    for (int i = 0; i < 4; ++i) glds16s((const unsigned char*)m + (size_t)(ks * 64 + 4 * i) * 8192, vo[i >> 1], dst + i * 1024);
  }
};

#define H_CLOB_W "a0", "a1", "a2", "a3", "a4", "a5", "a6", "a7", "a8", "a9", "a10", "a11", "a12", "a13", "a14", "a15", "a16", "a17", "a18", "a19", "a20", "a21", "a22", "a23", "a24", "a25", "a26", "a27", "a28", "a29", "a30", "a31", "a32", "a33", "a34", "a35", "a36", "a37", "a38", "a39", "a40", "a41", "a42", "a43", "a44", "a45", "a46", "a47", "a48", "a49", "a50", "a51", "a52", "a53", "a54", "a55", "a56", "a57", "a58", "a59", "a60", "a61", "a62", "a63", "a64", "a65", "a66", "a67", "a68", "a69", "a70", "a71", "a72", "a73", "a74", "a75", "a76", "a77", "a78", "a79", "a80", "a81", "a82", "a83", "a84", "a85", "a86", "a87", "a88", "a89", "a90", "a91", "a92", "a93", "a94", "a95", "a96", "a97", "a98", "a99", "a100", "a101", "a102", "a103", "a104", "a105", "a106", "a107", "a108", "a109", "a110", "a111", "a112", "a113", "a114", "a115", "a116", "a117", "a118", "a119", "a120", "a121", "a122", "a123", "a124", "a125", "a126", "a127", "a128", "a129", "a130", "a131", "a132", "a133", "a134", "a135", "a136", "a137", "a138", "a139", "a140", "a141", "a142", "a143", "a144", "a145", "a146", "a147", "a148", "a149", "a150", "a151", "a152", "a153", "a154", "a155", "a156", "a157", "a158", "a159", "a160", "a161", "a162", "a163", "a164", "a165", "a166", "a167", "a168", "a169", "a170", "a171", "a172", "a173", "a174", "a175", "a176", "a177", "a178", "a179", "a180", "a181", "a182", "a183", "a184", "a185", "a186", "a187", "a188", "a189", "a190", "a191", "a192", "a193", "a194", "a195", "a196", "a197", "a198", "a199", "a200", "a201", "a202", "a203", "a204", "a205", "a206", "a207", "a208", "a209", "a210", "a211", "a212", "a213", "a214", "a215", "a216", "a217", "a218", "a219", "a220", "a221", "a222", "a223", "a224", "a225", "a226", "a227", "a228", "a229", "a230", "a231", "a232", "a233", "a234", "a235", "a236", "a237", "a238", "a239", "a240", "a241", "a242", "a243", "a244", "a245", "a246", "a247", "a248", "a249", "a250", "a251", "a252", "a253", "a254", "a255", "v160", "v161", "v162", "v163", "v164", "v165", "v166", "v167", "v168", "v169", "v170", "v171", "v172", "v173", "v174", "v175", "v176", "v177", "v178", "v179", "v180", "v181", "v182", "v183", "v184", "v185", "v186", "v187", "v188", "v189", "v190", "v191", "v192", "v193", "v194", "v195", "v196", "v197", "v198", "v199", "v200", "v201", "v202", "v203", "v204", "v205", "v206", "v207", "v208", "v209", "v210", "v211", "v212", "v213", "v214", "v215", "v216", "v217", "v218", "v219", "v220", "v221", "v222", "v223", "v224", "v225", "v226", "v227", "v228", "v229", "v230", "v231", "v232", "v233", "v234", "v235", "v236", "v237", "v238", "v239", "v240", "v241", "v242", "v243", "v244", "v245", "v246", "v247", "v248", "v249", "v250", "v251", "v252", "v253", "v254", "v255"
#define H_CLOB_AD "v128", "v129", "v130", "v131", "v132", "v133", "v136", "v137", "v138", "v139", "v140", "v141", "v142", "v143", "v144", "v145", "v146", "v147", "v148", "v149", "v150", "v151", "v152", "v153", "v154", "v155", "v156", "v157", "v158", "v159"
#define H_CLOB_V "v0", "v1", "v2", "v3", "v4", "v5", "v6", "v7", "v8", "v9", "v10", "v11", "v12", "v13", "v14", "v15", "v16", "v17", "v18", "v19", "v20", "v21", "v22", "v23", "v24", "v25", "v26", "v27", "v28", "v29", "v30", "v31", "v32", "v33", "v34", "v35", "v36", "v37", "v38", "v39", "v40", "v41", "v42", "v43", "v44", "v45", "v46", "v47", "v48", "v49", "v50", "v51", "v52", "v53", "v54", "v55", "v56", "v57", "v58", "v59", "v60", "v61", "v62", "v63", "v64", "v65", "v66", "v67", "v68", "v69", "v70", "v71", "v72", "v73", "v74", "v75", "v76", "v77", "v78", "v79", "v80", "v81", "v82", "v83", "v84", "v85", "v86", "v87", "v88", "v89", "v90", "v91", "v92", "v93", "v94", "v95", "v96", "v97", "v98", "v99", "v100", "v101", "v102", "v103", "v104", "v105", "v106", "v107", "v108", "v109", "v110", "v111", "v112", "v113", "v114", "v115", "v116", "v117", "v118", "v119", "v120", "v121", "v122", "v123", "v124", "v125", "v126", "v127"
#define H_CLOB_S "s40", "s41", "s42", "s43", "s44", "s45", "s46", "s47", "s48", "s49", "s50", "s51", "s52", "s53", "s54", "s55", "s56", "s57", "s58", "s59", "s60", "s61", "s62", "s63", "s64", "s65", "s66", "s67", "s68", "s69", "s70", "s71", "s72", "s73", "s74", "s75", "s76", "s77", "s78", "s79", "s80", "s81", "s82", "s83", "s84", "s85", "s86", "s87", "s88", "s89", "s90", "s91", "s92", "s93", "s94", "s95", "s96", "s97", "s98", "s99"
#define H_LOOP_ASM \
  "s_cmp_eq_u32 s82, 0\n\t" \
  "s_cbranch_scc1 .LH0start%=\n\t" \
  "s_cmp_eq_u32 s82, 1\n\t" \
  "s_cbranch_scc1 .LH1start%=\n\t" \
  "s_cmp_eq_u32 s82, 2\n\t" \
  "s_cbranch_scc1 .LH2start%=\n\t" \
  "s_branch .LH3start%=\n\t" \
  ".LH0start%=:\n\t" \
  "global_load_dword v0, v[128:129], off\n\t" \
  "global_load_dword v1, v[128:129], off offset:8\n\t" \
  "global_load_dword v2, v[128:129], off offset:16\n\t" \
  "global_load_dword v3, v[128:129], off offset:24\n\t" \
  "global_load_dword v4, v[128:129], off offset:128\n\t" \
  "global_load_dword v5, v[128:129], off offset:136\n\t" \
  "global_load_dword v6, v[128:129], off offset:144\n\t" \
  "global_load_dword v7, v[128:129], off offset:152\n\t" \
  "ds_read_b128 v[52:55], v146\n\t" \
  "ds_read_b128 v[56:59], v146 offset:1024\n\t" \
  "ds_read_b128 v[60:63], v146 offset:2048\n\t" \
  "ds_read_b128 v[64:67], v146 offset:3072\n\t" \
  "ds_read_b128 v[68:71], v146 offset:4096\n\t" \
  "ds_read_b128 v[72:75], v146 offset:5120\n\t" \
  "ds_read_b128 v[76:79], v146 offset:6144\n\t" \
  "ds_read_b128 v[80:83], v146 offset:7168\n\t" \
  "v_mov_b64 v[32:33], 0\n\t" \
  "v_mov_b64 v[34:35], 0\n\t" \
  "v_mov_b64 v[36:37], 0\n\t" \
  "v_mov_b64 v[38:39], 0\n\t" \
  "v_mov_b64 v[40:41], 0\n\t" \
  "v_mov_b64 v[42:43], 0\n\t" \
  "v_mov_b64 v[44:45], 0\n\t" \
  "v_mov_b64 v[46:47], 0\n\t" \
  "v_mov_b64 v[48:49], 0\n\t" \
  "v_mov_b64 v[50:51], 0\n\t" \
  "v_mov_b32 v131, 0\n\t" \
  "v_mov_b32 v137, 0\n\t" \
  "v_mov_b32 v154, 0x3f803f80\n\t" \
  "v_mov_b32 v155, 0x3f803f80\n\t" \
  "v_mov_b32 v156, 0x3f803f80\n\t" \
  "v_mov_b32 v157, 0x3f803f80\n\t" \
  "s_waitcnt vmcnt(0)\n\t" \
  "v_mov_b32 v128, 0\n\t" \
  "s_memrealtime s[66:67]\n\t" \
  "s_waitcnt lgkmcnt(0)\n\t" \
  "s_mov_b32 s91, s66\n\t" \
  "s_add_u32 s66, s66, s88\n\t" \
  "s_or_b32 s66, s66, 1\n\t" \
  "v_mov_b32 v132, s66\n\t" \
  "s_mov_b32 s88, -8\n\t" \
  "s_mov_b32 s77, 16384\n\t" \
  "s_mov_b32 s75, s90\n\t" \
  "s_add_u32 s90, s90, 128\n\t" \
  "s_mov_b32 s74, s89\n\t" \
  "buffer_store_dword v132, v153, s[40:43], s75 offen sc1\n\t" \
  "s_nop 0\n\t" \
  "s_mov_b32 s73, s59\n\t" \
  "s_add_u32 s76, s76, s59\n\t" \
  "v_mov_b32 v129, 1\n\t" \
  "ds_write_b32 v139, v129 offset:45056\n\t" \
  "v_min_u32 v0, -2, v0\n\t" \
  "v_min_u32 v1, -2, v1\n\t" \
  "v_min_u32 v2, -2, v2\n\t" \
  "v_min_u32 v3, -2, v3\n\t" \
  "v_min_u32 v4, -2, v4\n\t" \
  "v_min_u32 v5, -2, v5\n\t" \
  "v_min_u32 v6, -2, v6\n\t" \
  "v_min_u32 v7, -2, v7\n\t" \
  "s_nop 1\n\t" \
  ".LH0top%=:\n\t" \
  "s_waitcnt vmcnt(6)\n\t" \
  ".LH0redo%=:\n\t" \
  "ds_write_b128 v139, v[0:3]\n\t" \
  "v_smfmac_f32_16x16x64_bf16 v[32:35], v[0:3], a[0:7], v138\n\t" \
  "ds_write_b128 v139, v[4:7] offset:1024\n\t" \
  "v_smfmac_f32_16x16x64_bf16 v[36:39], v[0:3], a[64:71], v138\n\t" \
  "v_max3_u32 v129, v0, v1, v2\n\t" \
  "v_max3_u32 v129, v129, v3, v4\n\t" \
  "v_smfmac_f32_16x16x64_bf16 v[40:43], v[0:3], a[128:135], v138\n\t" \
  "v_max3_u32 v129, v129, v5, v6\n\t" \
  "v_max_u32 v129, v129, v7\n\t" \
  "v_smfmac_f32_16x16x64_bf16 v[44:47], v[0:3], a[192:199], v138\n\t" \
  "v_cmp_eq_u32 vcc, -1, v129\n\t" \
  "s_cbranch_vccnz .LH0failb%=\n\t" \
  ".LH0okb%=:\n\t" \
  "s_waitcnt lgkmcnt(0)\n\t" \
  "s_barrier\n\t" \
  "ds_read_b128 v[8:11], v139 offset:2048\n\t" \
  "v_smfmac_f32_16x16x64_bf16 v[32:35], v[4:7], a[8:15], v138\n\t" \
  "ds_read_b128 v[12:15], v139 offset:3072\n\t" \
  "v_smfmac_f32_16x16x64_bf16 v[36:39], v[4:7], a[72:79], v138\n\t" \
  "ds_read_b128 v[16:19], v139 offset:4096\n\t" \
  "v_smfmac_f32_16x16x64_bf16 v[40:43], v[4:7], a[136:143], v138\n\t" \
  "ds_read_b128 v[20:23], v139 offset:5120\n\t" \
  "v_smfmac_f32_16x16x64_bf16 v[44:47], v[4:7], a[200:207], v138\n\t" \
  "ds_read_b128 v[24:27], v139 offset:6144\n\t" \
  "v_smfmac_f32_16x16x64_bf16 v[48:51], v[0:3], v[160:167], v138\n\t" \
  "ds_read_b128 v[28:31], v139 offset:7168\n\t" \
  "v_smfmac_f32_16x16x64_bf16 v[48:51], v[4:7], v[168:175], v138\n\t" \
  "s_waitcnt lgkmcnt(5)\n\t" \
  "v_smfmac_f32_16x16x64_bf16 v[32:35], v[8:11], a[16:23], v138\n\t" \
  "v_smfmac_f32_16x16x64_bf16 v[36:39], v[8:11], a[80:87], v138\n\t" \
  "s_waitcnt lgkmcnt(4)\n\t" \
  "v_smfmac_f32_16x16x64_bf16 v[32:35], v[12:15], a[24:31], v138\n\t" \
  "v_smfmac_f32_16x16x64_bf16 v[36:39], v[12:15], a[88:95], v138\n\t" \
  "s_waitcnt lgkmcnt(3)\n\t" \
  "v_smfmac_f32_16x16x64_bf16 v[32:35], v[16:19], a[32:39], v138\n\t" \
  "v_smfmac_f32_16x16x64_bf16 v[36:39], v[16:19], a[96:103], v138\n\t" \
  "s_waitcnt lgkmcnt(2)\n\t" \
  "v_smfmac_f32_16x16x64_bf16 v[32:35], v[20:23], a[40:47], v138\n\t" \
  "v_smfmac_f32_16x16x64_bf16 v[36:39], v[20:23], a[104:111], v138\n\t" \
  "s_waitcnt lgkmcnt(1)\n\t" \
  "v_smfmac_f32_16x16x64_bf16 v[32:35], v[24:27], a[48:55], v138\n\t" \
  "ds_read_b128 v[0:3], v146 offset:8192\n\t" \
  "v_smfmac_f32_16x16x64_bf16 v[36:39], v[24:27], a[112:119], v138\n\t" \
  "s_waitcnt lgkmcnt(1)\n\t" \
  "v_smfmac_f32_16x16x64_bf16 v[32:35], v[28:31], a[56:63], v138\n\t" \
  "ds_read_b128 v[4:7], v146 offset:9216\n\t" \
  "v_smfmac_f32_16x16x64_bf16 v[36:39], v[28:31], a[120:127], v138\n\t" \
  "v_smfmac_f32_16x16x64_bf16 v[48:51], v[8:11], v[176:183], v138\n\t" \
  "v_smfmac_f32_16x16x64_bf16 v[48:51], v[12:15], v[184:191], v138\n\t" \
  "s_nop 0\n\t" \
  "v_permlane32_swap_b32 v32, v36\n\t" \
  "v_smfmac_f32_16x16x64_bf16 v[48:51], v[16:19], v[192:199], v138\n\t" \
  "v_permlane32_swap_b32 v33, v37\n\t" \
  "v_smfmac_f32_16x16x64_bf16 v[48:51], v[20:23], v[200:207], v138\n\t" \
  "v_permlane32_swap_b32 v34, v38\n\t" \
  "v_smfmac_f32_16x16x64_bf16 v[48:51], v[24:27], v[208:215], v138\n\t" \
  "v_permlane32_swap_b32 v35, v39\n\t" \
  "v_smfmac_f32_16x16x64_bf16 v[48:51], v[28:31], v[216:223], v138\n\t" \
  "v_add_f32 v32, v32, v36\n\t" \
  "v_add_f32 v33, v33, v37\n\t" \
  "v_smfmac_f32_16x16x64_bf16 v[40:43], v[8:11], a[144:151], v138\n\t" \
  "v_add_f32 v34, v34, v38\n\t" \
  "v_add_f32 v35, v35, v39\n\t" \
  "v_smfmac_f32_16x16x64_bf16 v[44:47], v[8:11], a[208:215], v138\n\t" \
  "v_cvt_pk_bf16_f32 v32, v32, v33\n\t" \
  "v_cvt_pk_bf16_f32 v33, v34, v35\n\t" \
  "v_smfmac_f32_16x16x64_bf16 v[40:43], v[12:15], a[152:159], v138\n\t" \
  "ds_write_b64 v140, v[32:33]\n\t" \
  "v_smfmac_f32_16x16x64_bf16 v[44:47], v[12:15], a[216:223], v138\n\t" \
  "v_permlane32_swap_b32 v48, v49\n\t" \
  "v_smfmac_f32_16x16x64_bf16 v[40:43], v[16:19], a[160:167], v138\n\t" \
  "v_permlane32_swap_b32 v50, v51\n\t" \
  "v_smfmac_f32_16x16x64_bf16 v[44:47], v[16:19], a[224:231], v138\n\t" \
  "v_add_f32 v48, v48, v49\n\t" \
  "v_add_f32 v50, v50, v51\n\t" \
  "v_smfmac_f32_16x16x64_bf16 v[40:43], v[20:23], a[168:175], v138\n\t" \
  "v_cvt_pk_bf16_f32 v130, v48, v50\n\t" \
  "v_smfmac_f32_16x16x64_bf16 v[44:47], v[20:23], a[232:239], v138\n\t" \
  "s_waitcnt lgkmcnt(0)\n\t" \
  "s_barrier\n\t" \
  "ds_read_b64_tr_b16 v[84:85], v141\n\t" \
  "ds_read_b64_tr_b16 v[86:87], v141 offset:256\n\t" \
  "v_smfmac_f32_16x16x64_bf16 v[40:43], v[24:27], a[176:183], v138\n\t" \
  "ds_read_b64_tr_b16 v[88:89], v141 offset:512\n\t" \
  "ds_read_b64_tr_b16 v[90:91], v141 offset:768\n\t" \
  "v_smfmac_f32_16x16x64_bf16 v[44:47], v[24:27], a[240:247], v138\n\t" \
  "ds_read_b64_tr_b16 v[92:93], v141 offset:1024\n\t" \
  "ds_read_b64_tr_b16 v[94:95], v141 offset:1280\n\t" \
  "v_smfmac_f32_16x16x64_bf16 v[40:43], v[28:31], a[184:191], v138\n\t" \
  "ds_read_b64_tr_b16 v[96:97], v141 offset:1536\n\t" \
  "ds_read_b64_tr_b16 v[98:99], v141 offset:1792\n\t" \
  "v_smfmac_f32_16x16x64_bf16 v[44:47], v[28:31], a[248:255], v138\n\t" \
  "ds_write_b64 v142, v[130:131]\n\t" \
  "s_waitcnt lgkmcnt(7)\n\t" \
  "v_mfma_f32_16x16x32_bf16 v[116:119], v[52:55], v[84:87], 0\n\t" \
  "ds_read_b128 v[8:11], v146 offset:10240\n\t" \
  "v_mfma_f32_16x16x32_bf16 v[120:123], v[68:71], v[84:87], 0\n\t" \
  "v_permlane32_swap_b32 v40, v44\n\t" \
  "v_permlane32_swap_b32 v41, v45\n\t" \
  "s_waitcnt lgkmcnt(6)\n\t" \
  "v_mfma_f32_16x16x32_bf16 v[116:119], v[56:59], v[88:91], v[116:119]\n\t" \
  "v_permlane32_swap_b32 v42, v46\n\t" \
  "v_permlane32_swap_b32 v43, v47\n\t" \
  "v_mfma_f32_16x16x32_bf16 v[120:123], v[72:75], v[88:91], v[120:123]\n\t" \
  "v_add_f32 v40, v40, v44\n\t" \
  "v_add_f32 v41, v41, v45\n\t" \
  "s_waitcnt lgkmcnt(4)\n\t" \
  "v_mfma_f32_16x16x32_bf16 v[116:119], v[60:63], v[92:95], v[116:119]\n\t" \
  "v_add_f32 v42, v42, v46\n\t" \
  "v_add_f32 v43, v43, v47\n\t" \
  "v_mfma_f32_16x16x32_bf16 v[120:123], v[76:79], v[92:95], v[120:123]\n\t" \
  "v_cvt_pk_bf16_f32 v40, v40, v41\n\t" \
  "v_cvt_pk_bf16_f32 v41, v42, v43\n\t" \
  "s_waitcnt lgkmcnt(2)\n\t" \
  "v_mfma_f32_16x16x32_bf16 v[116:119], v[64:67], v[96:99], v[116:119]\n\t" \
  "ds_write_b64 v140, v[40:41] offset:2048\n\t" \
  "v_mfma_f32_16x16x32_bf16 v[120:123], v[80:83], v[96:99], v[120:123]\n\t" \
  "s_nop 7\n\t" \
  "v_mov_b32_dpp v116, v120 quad_perm:[0,1,2,3] row_mask:0xf bank_mask:0xc\n\t" \
  "v_mov_b32_dpp v117, v121 quad_perm:[0,1,2,3] row_mask:0xf bank_mask:0xc\n\t" \
  "v_mov_b32_dpp v118, v122 quad_perm:[0,1,2,3] row_mask:0xf bank_mask:0xc\n\t" \
  "v_mov_b32_dpp v119, v123 quad_perm:[0,1,2,3] row_mask:0xf bank_mask:0xc\n\t" \
  "v_max3_f32 v34, v116, v117, v118\n\t" \
  "v_max_f32 v34, v34, v119\n\t" \
  "s_cmp_eq_u32 s61, 63\n\t" \
  "s_waitcnt lgkmcnt(0)\n\t" \
  "s_barrier\n\t" \
  "s_cbranch_scc1 .LH0last%=\n\t" \
  "v_max_f32_dpp v34, v34, v34 row_ror:8 row_mask:0xf bank_mask:0xf\n\t" \
  "v_mov_b32 v33, v34\n\t" \
  "ds_read_b64_tr_b16 v[100:101], v141 offset:2048\n\t" \
  "ds_read_b64_tr_b16 v[102:103], v141 offset:2304\n\t" \
  "ds_read_b64_tr_b16 v[104:105], v141 offset:2560\n\t" \
  "ds_read_b64_tr_b16 v[106:107], v141 offset:2816\n\t" \
  "v_permlane32_swap_b32 v34, v33\n\t" \
  "v_max_f32 v34, v34, v33\n\t" \
  "v_mov_b32 v33, v34\n\t" \
  "ds_read_b64_tr_b16 v[108:109], v141 offset:3072\n\t" \
  "ds_read_b64_tr_b16 v[110:111], v141 offset:3328\n\t" \
  "ds_read_b64_tr_b16 v[112:113], v141 offset:3584\n\t" \
  "ds_read_b64_tr_b16 v[114:115], v141 offset:3840\n\t" \
  "ds_read_b128 v[12:15], v146 offset:11264\n\t" \
  "v_permlane16_swap_b32 v34, v33\n\t" \
  "v_max_f32 v34, v34, v33\n\t" \
  "v_mul_f32 v34, s81, v34\n\t" \
  "s_waitcnt lgkmcnt(7)\n\t" \
  "v_mfma_f32_16x16x32_bf16 v[124:127], v[100:103], v[84:87], 0\n\t" \
  "v_fma_f32 v120, v116, s81, -v34\n\t" \
  "v_fma_f32 v121, v117, s81, -v34\n\t" \
  "s_waitcnt lgkmcnt(5)\n\t" \
  "v_mfma_f32_16x16x32_bf16 v[124:127], v[104:107], v[88:91], v[124:127]\n\t" \
  "v_fma_f32 v122, v118, s81, -v34\n\t" \
  "v_fma_f32 v123, v119, s81, -v34\n\t" \
  "s_waitcnt lgkmcnt(3)\n\t" \
  "v_mfma_f32_16x16x32_bf16 v[124:127], v[108:111], v[92:95], v[124:127]\n\t" \
  "v_exp_f32 v120, v120\n\t" \
  "s_waitcnt lgkmcnt(1)\n\t" \
  "v_mfma_f32_16x16x32_bf16 v[124:127], v[112:115], v[96:99], v[124:127]\n\t" \
  "v_exp_f32 v121, v121\n\t" \
  "v_exp_f32 v122, v122\n\t" \
  "v_exp_f32 v123, v123\n\t" \
  "ds_read_b128 v[40:43], v143\n\t" \
  "v_cvt_pk_bf16_f32 v108, v120, v121\n\t" \
  "v_cvt_pk_bf16_f32 v109, v122, v123\n\t" \
  "v_mov_b32 v110, v108\n\t" \
  "v_mov_b32 v111, v109\n\t" \
  "v_mov_b32_dpp v108, v108 row_ror:8 row_mask:0xf bank_mask:0xc\n\t" \
  "v_mov_b32_dpp v109, v109 row_ror:8 row_mask:0xf bank_mask:0xc\n\t" \
  "v_mov_b32_dpp v110, v110 row_ror:8 row_mask:0xf bank_mask:0x3\n\t" \
  "v_mov_b32_dpp v111, v111 row_ror:8 row_mask:0xf bank_mask:0x3\n\t" \
  "v_mov_b32_dpp v124, v125 quad_perm:[0,1,2,3] row_mask:0xc bank_mask:0xf\n\t" \
  "v_mov_b32_dpp v126, v127 quad_perm:[0,1,2,3] row_mask:0xc bank_mask:0xf\n\t" \
  "v_mfma_f32_16x16x32_bf16 v[52:55], v[0:3], v[108:111], 0\n\t" \
  "ds_read_b128 v[44:47], v143 offset:16\n\t" \
  "v_mfma_f32_16x16x32_bf16 v[56:59], v[4:7], v[108:111], 0\n\t" \
  "s_waitcnt lgkmcnt(2)\n\t" \
  "v_mov_b32_dpp v124, v126 quad_perm:[0,1,2,3] row_mask:0xf bank_mask:0xc\n\t" \
  "v_fma_f32 v37, v124, s81, -v34\n\t" \
  "v_mfma_f32_16x16x32_bf16 v[60:63], v[8:11], v[108:111], 0\n\t" \
  "v_cmp_lt_f32 vcc, s79, v37\n\t" \
  "v_exp_f32 v37, v37\n\t" \
  "v_mfma_f32_16x16x32_bf16 v[64:67], v[12:15], v[108:111], 0\n\t" \
  "s_cbranch_vccnz .LH0bigm%=\n\t" \
  ".LH0bigr%=:\n\t" \
  "v_mov_b32 v38, v37\n\t" \
  "v_mfma_f32_16x16x32_bf16 v[76:79], v[108:111], v[154:157], 0\n\t" \
  "s_nop 0\n\t" \
  "v_mov_b32_dpp v37, v37 row_ror:8 row_mask:0xf bank_mask:0xc\n\t" \
  "v_mov_b32_dpp v38, v38 row_ror:8 row_mask:0xf bank_mask:0x3\n\t" \
  "v_cvt_pk_bf16_f32 v136, v37, v38\n\t" \
  "s_waitcnt lgkmcnt(0)\n\t" \
  "s_nop 0\n\t" \
  "v_mfma_f32_16x16x16_bf16 v[52:55], v[40:41], v[136:137], v[52:55]\n\t" \
  "s_mov_b64 exec, 1\n\t" \
  "buffer_load_dword v132, off, s[40:43], s75 sc1\n\t" \
  "s_mov_b64 exec, -1\n\t" \
  "v_mfma_f32_16x16x16_bf16 v[56:59], v[42:43], v[136:137], v[56:59]\n\t" \
  "v_mfma_f32_16x16x16_bf16 v[60:63], v[44:45], v[136:137], v[60:63]\n\t" \
  "v_mfma_f32_16x16x16_bf16 v[64:67], v[46:47], v[136:137], v[64:67]\n\t" \
  "v_cvt_pk_bf16_f32 v68, v52, v53\n\t" \
  "v_cvt_pk_bf16_f32 v69, v54, v55\n\t" \
  "v_mfma_f32_16x16x16_bf16 v[76:79], v[136:137], v[154:155], v[76:79]\n\t" \
  "v_cvt_pk_bf16_f32 v70, v56, v57\n\t" \
  "v_cvt_pk_bf16_f32 v71, v58, v59\n\t" \
  "v_cvt_pk_bf16_f32 v72, v60, v61\n\t" \
  "v_cvt_pk_bf16_f32 v73, v62, v63\n\t" \
  "v_mfma_f32_16x16x32_bf16 v[84:87], v[68:71], v[224:227], 0\n\t" \
  "v_cvt_pk_bf16_f32 v74, v64, v65\n\t" \
  "v_cvt_pk_bf16_f32 v75, v66, v67\n\t" \
  "v_mfma_f32_16x16x32_bf16 v[88:91], v[68:71], v[232:235], 0\n\t" \
  "s_add_u32 s68, s48, 65536\n\t" \
  "v_rcp_f32 v76, v76\n\t" \
  "v_mfma_f32_16x16x32_bf16 v[84:87], v[72:75], v[228:231], v[84:87]\n\t" \
  "v_rcp_f32 v77, v77\n\t" \
  "v_mfma_f32_16x16x32_bf16 v[88:91], v[72:75], v[236:239], v[88:91]\n\t" \
  "v_rcp_f32 v78, v78\n\t" \
  "v_mfma_f32_16x16x32_bf16 v[92:95], v[68:71], v[240:243], 0\n\t" \
  "v_rcp_f32 v79, v79\n\t" \
  "v_mfma_f32_16x16x32_bf16 v[96:99], v[68:71], v[248:251], 0\n\t" \
  "s_nop 0\n\t" \
  "v_mov_b32_dpp v84, v88 quad_perm:[0,1,2,3] row_mask:0xc bank_mask:0xf\n\t" \
  "v_mfma_f32_16x16x32_bf16 v[92:95], v[72:75], v[244:247], v[92:95]\n\t" \
  "v_mov_b32_dpp v85, v89 quad_perm:[0,1,2,3] row_mask:0xc bank_mask:0xf\n\t" \
  "v_mov_b32_dpp v86, v90 quad_perm:[0,1,2,3] row_mask:0xc bank_mask:0xf\n\t" \
  "v_mfma_f32_16x16x32_bf16 v[96:99], v[72:75], v[252:255], v[96:99]\n\t" \
  "v_mov_b32_dpp v87, v91 quad_perm:[0,1,2,3] row_mask:0xc bank_mask:0xf\n\t" \
  "v_mul_f32 v84, v84, v76\n\t" \
  "v_mul_f32 v85, v85, v77\n\t" \
  "v_mul_f32 v86, v86, v78\n\t" \
  "v_mul_f32 v87, v87, v79\n\t" \
  "v_cvt_pk_bf16_f32 v84, v84, v85\n\t" \
  "v_cvt_pk_bf16_f32 v85, v86, v87\n\t" \
  "buffer_store_dwordx2 v[84:85], v148, s[40:43], s48 offen sc1\n\t" \
  "v_mov_b32_dpp v92, v96 quad_perm:[0,1,2,3] row_mask:0xc bank_mask:0xf\n\t" \
  "v_mov_b32_dpp v93, v97 quad_perm:[0,1,2,3] row_mask:0xc bank_mask:0xf\n\t" \
  "v_mov_b32_dpp v94, v98 quad_perm:[0,1,2,3] row_mask:0xc bank_mask:0xf\n\t" \
  "v_mov_b32_dpp v95, v99 quad_perm:[0,1,2,3] row_mask:0xc bank_mask:0xf\n\t" \
  "v_mul_f32 v92, v92, v76\n\t" \
  "v_mul_f32 v93, v93, v77\n\t" \
  "v_mul_f32 v94, v94, v78\n\t" \
  "v_mul_f32 v95, v95, v79\n\t" \
  "v_cvt_pk_bf16_f32 v92, v92, v93\n\t" \
  "v_cvt_pk_bf16_f32 v93, v94, v95\n\t" \
  "buffer_store_dwordx2 v[92:93], v148, s[40:43], s68 offen sc1\n\t" \
  "s_memrealtime s[64:65]\n\t" \
  "ds_read_b64_tr_b16 v[8:9], v141 offset:2048\n\t" \
  "ds_read_b64_tr_b16 v[10:11], v141 offset:2304\n\t" \
  "ds_read_b128 v[120:123], v139 offset:15360\n\t" \
  "ds_read_b32 v129, v139 offset:45056\n\t" \
  "v_mov_b64 v[32:33], 0\n\t" \
  "v_mov_b64 v[34:35], 0\n\t" \
  "v_mov_b64 v[36:37], 0\n\t" \
  "v_mov_b64 v[38:39], 0\n\t" \
  "v_mov_b64 v[40:41], 0\n\t" \
  "v_mov_b64 v[42:43], 0\n\t" \
  "v_mov_b64 v[44:45], 0\n\t" \
  "v_mov_b64 v[46:47], 0\n\t" \
  "v_mov_b64 v[48:49], 0\n\t" \
  "v_mov_b64 v[50:51], 0\n\t" \
  "s_add_u32 s68, s48, s51\n\t" \
  "s_waitcnt vmcnt(2)\n\t" \
  "v_readfirstlane_b32 s70, v132\n\t" \
  "s_waitcnt lgkmcnt(0)\n\t" \
  "v_readfirstlane_b32 s72, v129\n\t" \
  "s_cmp_eq_u32 s61, 0\n\t" \
  "s_cselect_b32 s72, 0, s72\n\t" \
  "s_mov_b32 s92, s64\n\t" \
  "s_cmp_eq_u32 s70, 0\n\t" \
  "s_cbranch_scc1 .LH0tmwait%=\n\t" \
  ".LH0tmok%=:\n\t" \
  ".LH0w1%=:\n\t" \
  "s_memrealtime s[66:67]\n\t" \
  "s_waitcnt lgkmcnt(0)\n\t" \
  "s_sub_u32 s69, s66, s70\n\t" \
  "s_cmp_lt_i32 s69, 0\n\t" \
  "s_cbranch_scc1 .LH0w1%=\n\t" \
  "buffer_load_dwordx4 v[116:119], v139, s[40:43], s68 offen sc1\n\t" \
  "s_add_u32 s98, s70, s60\n\t" \
  "s_barrier\n\t" \
  "s_sub_u32 s69, s92, s91\n\t" \
  "s_mov_b32 s91, s98\n\t" \
  "s_sub_u32 s71, s61, s88\n\t" \
  "s_cmp_le_u32 s71, 3\n\t" \
  "s_cselect_b32 s71, 2, 0\n\t" \
  "s_min_u32 s69, s69, s76\n\t" \
  "s_cmp_lg_u32 s72, s61\n\t" \
  "s_cselect_b32 s73, s69, s73\n\t" \
  "s_cselect_b32 s76, s69, s76\n\t" \
  "s_cselect_b32 s88, s88, s61\n\t" \
  "s_cselect_b32 s71, 0, s71\n\t" \
  "s_add_u32 s93, s93, s71\n\t" \
  "s_add_u32 s69, s73, s93\n\t" \
  "s_add_u32 s69, s69, s98\n\t" \
  "s_max_u32 s69, s69, 1\n\t" \
  "v_mov_b32 v132, s69\n\t" \
  "buffer_store_dword v132, v153, s[40:43], s90 offen sc1\n\t" \
  "buffer_load_dword v128, off, s[44:47], s56 sc1\n\t" \
  "s_add_u32 s69, s61, 1\n\t" \
  "s_and_b32 s69, s69, 3\n\t" \
  "s_lshl_b32 s69, s69, 16\n\t" \
  "s_add_u32 s70, s69, s50\n\t" \
  "s_waitcnt vmcnt(2)\n\t" \
  ".LH0redoa%=:\n\t" \
  "v_mfma_f32_16x16x32_bf16 v[124:127], v[116:119], v[120:123], 0\n\t" \
  "s_nop 7\n\t" \
  "v_permlane32_swap_b32 v124, v125\n\t" \
  "v_permlane32_swap_b32 v126, v127\n\t" \
  "v_add_f32 v124, v124, v125\n\t" \
  "v_add_f32 v126, v126, v127\n\t" \
  "s_nop 1\n\t" \
  "v_permlane16_swap_b32 v124, v126\n\t" \
  "v_add_f32 v124, v124, v126\n\t" \
  "ds_write_b32 v144, v124\n\t" \
  "v_cmp_u_f32 vcc, v124, v124\n\t" \
  "s_cbranch_vccnz .LH0faila%=\n\t" \
  ".LH0oka%=:\n\t" \
  "s_waitcnt lgkmcnt(0)\n\t" \
  "s_barrier\n\t" \
  "ds_read_b128 v[124:127], v145\n\t" \
  "ds_read_b128 v[120:123], v145 offset:64\n\t" \
  "s_waitcnt lgkmcnt(1)\n\t" \
  "v_pk_add_f32 v[124:125], v[124:125], v[126:127]\n\t" \
  "s_waitcnt lgkmcnt(0)\n\t" \
  "v_pk_add_f32 v[120:121], v[120:121], v[122:123]\n\t" \
  "v_add_f32 v124, v124, v125\n\t" \
  "v_add_f32 v120, v120, v121\n\t" \
  "v_cvt_pk_bf16_f32 v124, v124, v120\n\t" \
  "buffer_store_dword v124, v149, s[40:43], s70 offen sc1\n\t" \
  "ds_read_b128 v[52:55], v147\n\t" \
  "ds_read_b128 v[56:59], v147 offset:1024\n\t" \
  "ds_read_b128 v[60:63], v147 offset:2048\n\t" \
  "ds_read_b128 v[64:67], v147 offset:3072\n\t" \
  "ds_read_b128 v[68:71], v147 offset:4096\n\t" \
  "ds_read_b128 v[72:75], v147 offset:5120\n\t" \
  "ds_read_b128 v[76:79], v147 offset:6144\n\t" \
  "ds_read_b128 v[80:83], v147 offset:7168\n\t" \
  "v_mov_b64 v[116:117], -1\n\t" \
  "v_mov_b64 v[118:119], -1\n\t" \
  "s_add_u32 s70, s70, s52\n\t" \
  ".LH0w2%=:\n\t" \
  "s_memrealtime s[66:67]\n\t" \
  "s_waitcnt lgkmcnt(0)\n\t" \
  "s_sub_u32 s71, s66, s98\n\t" \
  "s_cmp_lt_i32 s71, 0\n\t" \
  "s_cbranch_scc1 .LH0w2%=\n\t" \
  "buffer_load_dwordx4 v[0:3], v139, s[40:43], s70 offen sc1\n\t" \
  "buffer_load_dwordx4 v[4:7], v139, s[40:43], s70 offen offset:1024 sc1\n\t" \
  "s_barrier\n\t" \
  "buffer_store_dwordx4 v[116:119], v139, s[40:43], s68 offen sc1\n\t" \
  "buffer_store_dword v130, v150, s[40:43], s57 offen sc1\n\t" \
  "buffer_store_dwordx4 v[8:11], v151, s[40:43], s58 offen sc1\n\t" \
  "s_mov_b32 s75, s90\n\t" \
  "s_add_u32 s90, s90, 128\n\t" \
  "s_add_u32 s68, s61, 4\n\t" \
  "s_lshr_b32 s68, s68, 1\n\t" \
  "s_cmp_ge_u32 s61, 62\n\t" \
  "s_cselect_b32 s68, 0, s68\n\t" \
  "s_waitcnt vmcnt(5)\n\t" \
  "v_readfirstlane_b32 s69, v128\n\t" \
  "s_cmp_lt_u32 s69, s68\n\t" \
  "s_cbranch_scc1 .LH0kvwait%=\n\t" \
  ".LH0kvok%=:\n\t" \
  "s_add_u32 m0, s77, 0\n\t" \
  "s_nop 0\n\t" \
  "buffer_load_dwordx4 v139, s[40:43], s53 offen sc1 lds\n\t" \
  "s_add_u32 m0, s77, 1024\n\t" \
  "s_nop 0\n\t" \
  "buffer_load_dwordx4 v139, s[40:43], s54 offen sc1 lds\n\t" \
  "s_add_u32 m0, s77, 2048\n\t" \
  "s_nop 0\n\t" \
  "buffer_load_dwordx4 v139, s[40:43], s55 offen sc1 lds\n\t" \
  "s_xor_b32 s77, s77, 0xc000\n\t" \
  "s_xor_b32 s56, s56, 128\n\t" \
  "s_and_b32 s69, s61, 3\n\t" \
  "s_cmp_eq_u32 s69, 1\n\t" \
  "s_cselect_b32 s71, 0x2000, 0\n\t" \
  "s_add_u32 s53, s53, 0x80000\n\t" \
  "s_xor_b32 s53, s53, s71\n\t" \
  "s_add_u32 s54, s54, 0x80000\n\t" \
  "s_xor_b32 s54, s54, s71\n\t" \
  "s_add_u32 s55, s55, 0x80000\n\t" \
  "s_xor_b32 s55, s55, s71\n\t" \
  "s_cmp_eq_u32 s69, 3\n\t" \
  "s_cselect_b32 s71, 0x2000, 0\n\t" \
  "s_add_u32 s57, s57, 0x80000\n\t" \
  "s_xor_b32 s57, s57, s71\n\t" \
  "s_add_u32 s58, s58, 0x80000\n\t" \
  "s_xor_b32 s58, s58, s71\n\t" \
  "v_swap_b32 v146, v147\n\t" \
  "s_mov_b32 s71, s48\n\t" \
  "s_mov_b32 s48, s49\n\t" \
  "s_mov_b32 s49, s71\n\t" \
  "s_add_u32 s61, s61, 1\n\t" \
  "s_branch .LH0top%=\n\t" \
  ".LH0faila%=:\n\t" \
  "v_max3_u32 v129, v116, v117, v118\n\t" \
  "v_max_u32 v129, v129, v119\n\t" \
  "v_cmp_eq_u32 vcc, -1, v129\n\t" \
  "s_cbranch_vccz .LH0oka%=\n\t" \
  "s_add_u32 s83, s83, 1\n\t" \
  "s_add_u32 s71, s61, 1\n\t" \
  "v_mov_b32 v129, s71\n\t" \
  "ds_write_b32 v139, v129 offset:45056\n\t" \
  "s_cmp_lg_u32 s62, 0\n\t" \
  "s_cbranch_scc1 .LH0oka%=\n\t" \
  "s_add_u32 s63, s63, 1\n\t" \
  "s_and_b32 s71, s63, 255\n\t" \
  "s_cmp_lg_u32 s71, 0\n\t" \
  "s_cbranch_scc1 .LH0sga%=\n\t" \
  "buffer_load_dword v129, off, s[44:47], 0 sc1\n\t" \
  "s_waitcnt vmcnt(0)\n\t" \
  "v_readfirstlane_b32 s62, v129\n\t" \
  "s_cmp_lt_u32 s63, 0x100000\n\t" \
  "s_cbranch_scc1 .LH0sga%=\n\t" \
  "s_mov_b32 s62, 1\n\t" \
  "v_mov_b32 v129, 1\n\t" \
  "buffer_store_dword v129, off, s[44:47], 0 sc1\n\t" \
  ".LH0sga%=:\n\t" \
  "s_sleep 3\n\t" \
  "buffer_load_dwordx4 v[116:119], v139, s[40:43], s68 offen sc1\n\t" \
  "s_memrealtime s[66:67]\n\t" \
  "s_waitcnt lgkmcnt(0)\n\t" \
  "s_add_u32 s69, s66, s60\n\t" \
  "s_add_u32 s69, s69, s73\n\t" \
  "s_add_u32 s69, s69, s93\n\t" \
  "s_add_u32 s69, s69, s74\n\t" \
  "s_max_u32 s69, s69, 1\n\t" \
  "v_mov_b32 v132, s69\n\t" \
  "buffer_store_dword v132, v153, s[40:43], s90 offen sc1\n\t" \
  "s_waitcnt vmcnt(0)\n\t" \
  "s_branch .LH0redoa%=\n\t" \
  ".LH0failb%=:\n\t" \
  "s_add_u32 s84, s84, 1\n\t" \
  "v_mov_b32 v129, s61\n\t" \
  "ds_write_b32 v139, v129 offset:45056\n\t" \
  "s_cmp_lg_u32 s62, 0\n\t" \
  "s_cbranch_scc1 .LH0okb%=\n\t" \
  "s_add_u32 s63, s63, 1\n\t" \
  "s_and_b32 s71, s63, 255\n\t" \
  "s_cmp_lg_u32 s71, 0\n\t" \
  "s_cbranch_scc1 .LH0sgb%=\n\t" \
  "buffer_load_dword v129, off, s[44:47], 0 sc1\n\t" \
  "s_waitcnt vmcnt(0)\n\t" \
  "v_readfirstlane_b32 s62, v129\n\t" \
  "s_cmp_lt_u32 s63, 0x100000\n\t" \
  "s_cbranch_scc1 .LH0sgb%=\n\t" \
  "s_mov_b32 s62, 1\n\t" \
  "v_mov_b32 v129, 1\n\t" \
  "buffer_store_dword v129, off, s[44:47], 0 sc1\n\t" \
  ".LH0sgb%=:\n\t" \
  "s_sleep 3\n\t" \
  "buffer_load_dwordx4 v[0:3], v139, s[40:43], s70 offen sc1\n\t" \
  "buffer_load_dwordx4 v[4:7], v139, s[40:43], s70 offen offset:1024 sc1\n\t" \
  "s_memrealtime s[66:67]\n\t" \
  "s_waitcnt lgkmcnt(0)\n\t" \
  "s_add_u32 s69, s66, s73\n\t" \
  "s_add_u32 s69, s69, s93\n\t" \
  "s_add_u32 s69, s69, s74\n\t" \
  "s_max_u32 s69, s69, 1\n\t" \
  "v_mov_b32 v132, s69\n\t" \
  "buffer_store_dword v132, v153, s[40:43], s75 offen sc1\n\t" \
  "s_nop 8\n\t" \
  "v_mov_b64 v[32:33], 0\n\t" \
  "v_mov_b64 v[34:35], 0\n\t" \
  "v_mov_b64 v[36:37], 0\n\t" \
  "v_mov_b64 v[38:39], 0\n\t" \
  "v_mov_b64 v[40:41], 0\n\t" \
  "v_mov_b64 v[42:43], 0\n\t" \
  "v_mov_b64 v[44:45], 0\n\t" \
  "v_mov_b64 v[46:47], 0\n\t" \
  "s_waitcnt vmcnt(0)\n\t" \
  "s_branch .LH0redo%=\n\t" \
  ".LH0bigm%=:\n\t" \
  "v_mul_f32 v37, s81, v124\n\t" \
  "v_max_f32 v34, v34, v37\n\t" \
  "s_nop 1\n\t" \
  "v_max_f32_dpp v34, v34, v34 row_ror:8 row_mask:0xf bank_mask:0xf\n\t" \
  "v_mov_b32 v33, v34\n\t" \
  "s_nop 1\n\t" \
  "v_permlane32_swap_b32 v34, v33\n\t" \
  "v_max_f32 v34, v34, v33\n\t" \
  "v_mov_b32 v33, v34\n\t" \
  "s_nop 1\n\t" \
  "v_permlane16_swap_b32 v34, v33\n\t" \
  "v_max_f32 v34, v34, v33\n\t" \
  "v_fma_f32 v120, v116, s81, -v34\n\t" \
  "v_fma_f32 v121, v117, s81, -v34\n\t" \
  "v_fma_f32 v122, v118, s81, -v34\n\t" \
  "v_fma_f32 v123, v119, s81, -v34\n\t" \
  "v_fma_f32 v37, v124, s81, -v34\n\t" \
  "v_exp_f32 v120, v120\n\t" \
  "v_exp_f32 v121, v121\n\t" \
  "v_exp_f32 v122, v122\n\t" \
  "v_exp_f32 v123, v123\n\t" \
  "v_exp_f32 v37, v37\n\t" \
  "v_cvt_pk_bf16_f32 v108, v120, v121\n\t" \
  "v_cvt_pk_bf16_f32 v109, v122, v123\n\t" \
  "v_mov_b32 v110, v108\n\t" \
  "v_mov_b32 v111, v109\n\t" \
  "v_mov_b32_dpp v108, v108 row_ror:8 row_mask:0xf bank_mask:0xc\n\t" \
  "v_mov_b32_dpp v109, v109 row_ror:8 row_mask:0xf bank_mask:0xc\n\t" \
  "v_mov_b32_dpp v110, v110 row_ror:8 row_mask:0xf bank_mask:0x3\n\t" \
  "v_mov_b32_dpp v111, v111 row_ror:8 row_mask:0xf bank_mask:0x3\n\t" \
  "s_nop 7\n\t" \
  "v_mfma_f32_16x16x32_bf16 v[52:55], v[0:3], v[108:111], 0\n\t" \
  "v_mfma_f32_16x16x32_bf16 v[56:59], v[4:7], v[108:111], 0\n\t" \
  "v_mfma_f32_16x16x32_bf16 v[60:63], v[8:11], v[108:111], 0\n\t" \
  "v_mfma_f32_16x16x32_bf16 v[64:67], v[12:15], v[108:111], 0\n\t" \
  "s_branch .LH0bigr%=\n\t" \
  ".LH0kvwait%=:\n\t" \
  "buffer_load_dword v128, off, s[44:47], s56 sc1\n\t" \
  "s_waitcnt vmcnt(0)\n\t" \
  "v_readfirstlane_b32 s69, v128\n\t" \
  "s_cmp_ge_u32 s69, s68\n\t" \
  "s_cbranch_scc1 .LH0kvok%=\n\t" \
  "s_sleep 4\n\t" \
  "s_cmp_lg_u32 s62, 0\n\t" \
  "s_cbranch_scc1 .LH0kvok%=\n\t" \
  "s_add_u32 s63, s63, 1\n\t" \
  "s_and_b32 s71, s63, 255\n\t" \
  "s_cmp_lg_u32 s71, 0\n\t" \
  "s_cbranch_scc1 .LH0sgk%=\n\t" \
  "buffer_load_dword v129, off, s[44:47], 0 sc1\n\t" \
  "s_waitcnt vmcnt(0)\n\t" \
  "v_readfirstlane_b32 s62, v129\n\t" \
  "s_cmp_lt_u32 s63, 0x100000\n\t" \
  "s_cbranch_scc1 .LH0sgk%=\n\t" \
  "s_mov_b32 s62, 1\n\t" \
  "v_mov_b32 v129, 1\n\t" \
  "buffer_store_dword v129, off, s[44:47], 0 sc1\n\t" \
  ".LH0sgk%=:\n\t" \
  "s_branch .LH0kvwait%=\n\t" \
  ".LH0tmwait%=:\n\t" \
  "buffer_load_dword v132, off, s[40:43], s75 sc1\n\t" \
  "s_waitcnt vmcnt(0)\n\t" \
  "v_readfirstlane_b32 s70, v132\n\t" \
  "s_cmp_lg_u32 s70, 0\n\t" \
  "s_cbranch_scc1 .LH0tmok%=\n\t" \
  "s_mov_b32 s70, s92\n\t" \
  "s_cmp_lg_u32 s62, 0\n\t" \
  "s_cbranch_scc1 .LH0tmok%=\n\t" \
  "s_add_u32 s63, s63, 1\n\t" \
  "s_and_b32 s71, s63, 255\n\t" \
  "s_cmp_lg_u32 s71, 0\n\t" \
  "s_cbranch_scc1 .LH0sgt%=\n\t" \
  "buffer_load_dword v129, off, s[44:47], 0 sc1\n\t" \
  "s_waitcnt vmcnt(0)\n\t" \
  "v_readfirstlane_b32 s62, v129\n\t" \
  "s_cmp_lt_u32 s63, 0x100000\n\t" \
  "s_cbranch_scc1 .LH0sgt%=\n\t" \
  "s_mov_b32 s62, 1\n\t" \
  "v_mov_b32 v129, 1\n\t" \
  "buffer_store_dword v129, off, s[44:47], 0 sc1\n\t" \
  ".LH0sgt%=:\n\t" \
  "s_branch .LH0tmwait%=\n\t" \
  ".LH0last%=:\n\t" \
  "ds_read_b64_tr_b16 v[8:9], v141 offset:2048\n\t" \
  "ds_read_b64_tr_b16 v[10:11], v141 offset:2304\n\t" \
  "s_waitcnt lgkmcnt(0)\n\t" \
  "buffer_store_dword v130, v150, s[40:43], s57 offen sc1\n\t" \
  "buffer_store_dwordx4 v[8:11], v151, s[40:43], s58 offen sc1\n\t" \
  "s_waitcnt vmcnt(0)\n\t" \
  "s_barrier\n\t" \
  "s_branch .LHend%=\n\t" \
  ".LH1start%=:\n\t" \
  "global_load_dword v0, v[128:129], off\n\t" \
  "global_load_dword v1, v[128:129], off offset:8\n\t" \
  "global_load_dword v2, v[128:129], off offset:16\n\t" \
  "global_load_dword v3, v[128:129], off offset:24\n\t" \
  "global_load_dword v4, v[128:129], off offset:128\n\t" \
  "global_load_dword v5, v[128:129], off offset:136\n\t" \
  "global_load_dword v6, v[128:129], off offset:144\n\t" \
  "global_load_dword v7, v[128:129], off offset:152\n\t" \
  "ds_read_b128 v[52:55], v146\n\t" \
  "ds_read_b128 v[56:59], v146 offset:1024\n\t" \
  "ds_read_b128 v[60:63], v146 offset:2048\n\t" \
  "ds_read_b128 v[64:67], v146 offset:3072\n\t" \
  "ds_read_b128 v[68:71], v146 offset:4096\n\t" \
  "ds_read_b128 v[72:75], v146 offset:5120\n\t" \
  "ds_read_b128 v[76:79], v146 offset:6144\n\t" \
  "ds_read_b128 v[80:83], v146 offset:7168\n\t" \
  "v_mov_b64 v[32:33], 0\n\t" \
  "v_mov_b64 v[34:35], 0\n\t" \
  "v_mov_b64 v[36:37], 0\n\t" \
  "v_mov_b64 v[38:39], 0\n\t" \
  "v_mov_b64 v[40:41], 0\n\t" \
  "v_mov_b64 v[42:43], 0\n\t" \
  "v_mov_b64 v[44:45], 0\n\t" \
  "v_mov_b64 v[46:47], 0\n\t" \
  "v_mov_b64 v[48:49], 0\n\t" \
  "v_mov_b64 v[50:51], 0\n\t" \
  "v_mov_b32 v131, 0\n\t" \
  "v_mov_b32 v137, 0\n\t" \
  "v_mov_b32 v154, 0x3f803f80\n\t" \
  "v_mov_b32 v155, 0x3f803f80\n\t" \
  "v_mov_b32 v156, 0x3f803f80\n\t" \
  "v_mov_b32 v157, 0x3f803f80\n\t" \
  "s_waitcnt vmcnt(0)\n\t" \
  "v_mov_b32 v128, 0\n\t" \
  "s_memrealtime s[66:67]\n\t" \
  "s_waitcnt lgkmcnt(0)\n\t" \
  "s_mov_b32 s91, s66\n\t" \
  "s_add_u32 s66, s66, s88\n\t" \
  "s_or_b32 s66, s66, 1\n\t" \
  "v_mov_b32 v132, s66\n\t" \
  "s_mov_b32 s88, -8\n\t" \
  "s_mov_b32 s77, 16384\n\t" \
  "s_mov_b32 s75, s90\n\t" \
  "s_add_u32 s90, s90, 128\n\t" \
  "s_mov_b32 s74, s89\n\t" \
  "s_nop 0\n\t" \
  "s_mov_b32 s73, s59\n\t" \
  "s_add_u32 s76, s76, s59\n\t" \
  "v_min_u32 v0, -2, v0\n\t" \
  "v_min_u32 v1, -2, v1\n\t" \
  "v_min_u32 v2, -2, v2\n\t" \
  "v_min_u32 v3, -2, v3\n\t" \
  "v_min_u32 v4, -2, v4\n\t" \
  "v_min_u32 v5, -2, v5\n\t" \
  "v_min_u32 v6, -2, v6\n\t" \
  "v_min_u32 v7, -2, v7\n\t" \
  "s_nop 1\n\t" \
  ".LH1top%=:\n\t" \
  "s_waitcnt vmcnt(7)\n\t" \
  ".LH1redo%=:\n\t" \
  "ds_write_b128 v139, v[0:3] offset:2048\n\t" \
  "v_smfmac_f32_16x16x64_bf16 v[32:35], v[0:3], a[16:23], v138\n\t" \
  "ds_write_b128 v139, v[4:7] offset:3072\n\t" \
  "v_smfmac_f32_16x16x64_bf16 v[36:39], v[0:3], a[80:87], v138\n\t" \
  "v_max3_u32 v129, v0, v1, v2\n\t" \
  "v_max3_u32 v129, v129, v3, v4\n\t" \
  "v_smfmac_f32_16x16x64_bf16 v[40:43], v[0:3], a[144:151], v138\n\t" \
  "v_max3_u32 v129, v129, v5, v6\n\t" \
  "v_max_u32 v129, v129, v7\n\t" \
  "v_smfmac_f32_16x16x64_bf16 v[44:47], v[0:3], a[208:215], v138\n\t" \
  "v_cmp_eq_u32 vcc, -1, v129\n\t" \
  "s_cbranch_vccnz .LH1failb%=\n\t" \
  ".LH1okb%=:\n\t" \
  "s_waitcnt lgkmcnt(0)\n\t" \
  "s_barrier\n\t" \
  "ds_read_b128 v[8:11], v139 offset:4096\n\t" \
  "v_smfmac_f32_16x16x64_bf16 v[32:35], v[4:7], a[24:31], v138\n\t" \
  "ds_read_b128 v[12:15], v139 offset:5120\n\t" \
  "v_smfmac_f32_16x16x64_bf16 v[36:39], v[4:7], a[88:95], v138\n\t" \
  "ds_read_b128 v[16:19], v139 offset:6144\n\t" \
  "v_smfmac_f32_16x16x64_bf16 v[40:43], v[4:7], a[152:159], v138\n\t" \
  "ds_read_b128 v[20:23], v139 offset:7168\n\t" \
  "v_smfmac_f32_16x16x64_bf16 v[44:47], v[4:7], a[216:223], v138\n\t" \
  "ds_read_b128 v[24:27], v139\n\t" \
  "v_smfmac_f32_16x16x64_bf16 v[48:51], v[0:3], v[176:183], v138\n\t" \
  "ds_read_b128 v[28:31], v139 offset:1024\n\t" \
  "v_smfmac_f32_16x16x64_bf16 v[48:51], v[4:7], v[184:191], v138\n\t" \
  "s_waitcnt lgkmcnt(5)\n\t" \
  "v_smfmac_f32_16x16x64_bf16 v[32:35], v[8:11], a[32:39], v138\n\t" \
  "v_smfmac_f32_16x16x64_bf16 v[36:39], v[8:11], a[96:103], v138\n\t" \
  "s_waitcnt lgkmcnt(4)\n\t" \
  "v_smfmac_f32_16x16x64_bf16 v[32:35], v[12:15], a[40:47], v138\n\t" \
  "v_smfmac_f32_16x16x64_bf16 v[36:39], v[12:15], a[104:111], v138\n\t" \
  "s_waitcnt lgkmcnt(3)\n\t" \
  "v_smfmac_f32_16x16x64_bf16 v[32:35], v[16:19], a[48:55], v138\n\t" \
  "v_smfmac_f32_16x16x64_bf16 v[36:39], v[16:19], a[112:119], v138\n\t" \
  "s_waitcnt lgkmcnt(2)\n\t" \
  "v_smfmac_f32_16x16x64_bf16 v[32:35], v[20:23], a[56:63], v138\n\t" \
  "v_smfmac_f32_16x16x64_bf16 v[36:39], v[20:23], a[120:127], v138\n\t" \
  "s_waitcnt lgkmcnt(1)\n\t" \
  "v_smfmac_f32_16x16x64_bf16 v[32:35], v[24:27], a[0:7], v138\n\t" \
  "ds_read_b128 v[0:3], v146 offset:8192\n\t" \
  "v_smfmac_f32_16x16x64_bf16 v[36:39], v[24:27], a[64:71], v138\n\t" \
  "s_waitcnt lgkmcnt(1)\n\t" \
  "v_smfmac_f32_16x16x64_bf16 v[32:35], v[28:31], a[8:15], v138\n\t" \
  "ds_read_b128 v[4:7], v146 offset:9216\n\t" \
  "v_smfmac_f32_16x16x64_bf16 v[36:39], v[28:31], a[72:79], v138\n\t" \
  "v_smfmac_f32_16x16x64_bf16 v[48:51], v[8:11], v[192:199], v138\n\t" \
  "v_smfmac_f32_16x16x64_bf16 v[48:51], v[12:15], v[200:207], v138\n\t" \
  "s_nop 0\n\t" \
  "v_permlane32_swap_b32 v32, v36\n\t" \
  "v_smfmac_f32_16x16x64_bf16 v[48:51], v[16:19], v[208:215], v138\n\t" \
  "v_permlane32_swap_b32 v33, v37\n\t" \
  "v_smfmac_f32_16x16x64_bf16 v[48:51], v[20:23], v[216:223], v138\n\t" \
  "v_permlane32_swap_b32 v34, v38\n\t" \
  "v_smfmac_f32_16x16x64_bf16 v[48:51], v[24:27], v[160:167], v138\n\t" \
  "v_permlane32_swap_b32 v35, v39\n\t" \
  "v_smfmac_f32_16x16x64_bf16 v[48:51], v[28:31], v[168:175], v138\n\t" \
  "v_add_f32 v32, v32, v36\n\t" \
  "v_add_f32 v33, v33, v37\n\t" \
  "v_smfmac_f32_16x16x64_bf16 v[40:43], v[8:11], a[160:167], v138\n\t" \
  "v_add_f32 v34, v34, v38\n\t" \
  "v_add_f32 v35, v35, v39\n\t" \
  "v_smfmac_f32_16x16x64_bf16 v[44:47], v[8:11], a[224:231], v138\n\t" \
  "v_cvt_pk_bf16_f32 v32, v32, v33\n\t" \
  "v_cvt_pk_bf16_f32 v33, v34, v35\n\t" \
  "v_smfmac_f32_16x16x64_bf16 v[40:43], v[12:15], a[168:175], v138\n\t" \
  "ds_write_b64 v140, v[32:33]\n\t" \
  "v_smfmac_f32_16x16x64_bf16 v[44:47], v[12:15], a[232:239], v138\n\t" \
  "v_permlane32_swap_b32 v48, v49\n\t" \
  "v_smfmac_f32_16x16x64_bf16 v[40:43], v[16:19], a[176:183], v138\n\t" \
  "v_permlane32_swap_b32 v50, v51\n\t" \
  "v_smfmac_f32_16x16x64_bf16 v[44:47], v[16:19], a[240:247], v138\n\t" \
  "v_add_f32 v48, v48, v49\n\t" \
  "v_add_f32 v50, v50, v51\n\t" \
  "v_smfmac_f32_16x16x64_bf16 v[40:43], v[20:23], a[184:191], v138\n\t" \
  "v_cvt_pk_bf16_f32 v130, v48, v50\n\t" \
  "v_smfmac_f32_16x16x64_bf16 v[44:47], v[20:23], a[248:255], v138\n\t" \
  "s_waitcnt lgkmcnt(0)\n\t" \
  "s_barrier\n\t" \
  "ds_read_b64_tr_b16 v[84:85], v141\n\t" \
  "ds_read_b64_tr_b16 v[86:87], v141 offset:256\n\t" \
  "v_smfmac_f32_16x16x64_bf16 v[40:43], v[24:27], a[128:135], v138\n\t" \
  "ds_read_b64_tr_b16 v[88:89], v141 offset:512\n\t" \
  "ds_read_b64_tr_b16 v[90:91], v141 offset:768\n\t" \
  "v_smfmac_f32_16x16x64_bf16 v[44:47], v[24:27], a[192:199], v138\n\t" \
  "ds_read_b64_tr_b16 v[92:93], v141 offset:1024\n\t" \
  "ds_read_b64_tr_b16 v[94:95], v141 offset:1280\n\t" \
  "v_smfmac_f32_16x16x64_bf16 v[40:43], v[28:31], a[136:143], v138\n\t" \
  "ds_read_b64_tr_b16 v[96:97], v141 offset:1536\n\t" \
  "ds_read_b64_tr_b16 v[98:99], v141 offset:1792\n\t" \
  "v_smfmac_f32_16x16x64_bf16 v[44:47], v[28:31], a[200:207], v138\n\t" \
  "ds_write_b64 v142, v[130:131]\n\t" \
  "s_waitcnt lgkmcnt(7)\n\t" \
  "v_mfma_f32_16x16x32_bf16 v[116:119], v[52:55], v[84:87], 0\n\t" \
  "ds_read_b128 v[8:11], v146 offset:10240\n\t" \
  "v_mfma_f32_16x16x32_bf16 v[120:123], v[68:71], v[84:87], 0\n\t" \
  "v_permlane32_swap_b32 v40, v44\n\t" \
  "v_permlane32_swap_b32 v41, v45\n\t" \
  "s_waitcnt lgkmcnt(6)\n\t" \
  "v_mfma_f32_16x16x32_bf16 v[116:119], v[56:59], v[88:91], v[116:119]\n\t" \
  "v_permlane32_swap_b32 v42, v46\n\t" \
  "v_permlane32_swap_b32 v43, v47\n\t" \
  "v_mfma_f32_16x16x32_bf16 v[120:123], v[72:75], v[88:91], v[120:123]\n\t" \
  "v_add_f32 v40, v40, v44\n\t" \
  "v_add_f32 v41, v41, v45\n\t" \
  "s_waitcnt lgkmcnt(4)\n\t" \
  "v_mfma_f32_16x16x32_bf16 v[116:119], v[60:63], v[92:95], v[116:119]\n\t" \
  "v_add_f32 v42, v42, v46\n\t" \
  "v_add_f32 v43, v43, v47\n\t" \
  "v_mfma_f32_16x16x32_bf16 v[120:123], v[76:79], v[92:95], v[120:123]\n\t" \
  "v_cvt_pk_bf16_f32 v40, v40, v41\n\t" \
  "v_cvt_pk_bf16_f32 v41, v42, v43\n\t" \
  "s_waitcnt lgkmcnt(2)\n\t" \
  "v_mfma_f32_16x16x32_bf16 v[116:119], v[64:67], v[96:99], v[116:119]\n\t" \
  "ds_write_b64 v140, v[40:41] offset:2048\n\t" \
  "v_mfma_f32_16x16x32_bf16 v[120:123], v[80:83], v[96:99], v[120:123]\n\t" \
  "s_nop 7\n\t" \
  "v_mov_b32_dpp v116, v120 quad_perm:[0,1,2,3] row_mask:0xf bank_mask:0xc\n\t" \
  "v_mov_b32_dpp v117, v121 quad_perm:[0,1,2,3] row_mask:0xf bank_mask:0xc\n\t" \
  "v_mov_b32_dpp v118, v122 quad_perm:[0,1,2,3] row_mask:0xf bank_mask:0xc\n\t" \
  "v_mov_b32_dpp v119, v123 quad_perm:[0,1,2,3] row_mask:0xf bank_mask:0xc\n\t" \
  "v_max3_f32 v34, v116, v117, v118\n\t" \
  "v_max_f32 v34, v34, v119\n\t" \
  "s_cmp_eq_u32 s61, 63\n\t" \
  "s_waitcnt lgkmcnt(0)\n\t" \
  "s_barrier\n\t" \
  "s_cbranch_scc1 .LH1last%=\n\t" \
  "v_max_f32_dpp v34, v34, v34 row_ror:8 row_mask:0xf bank_mask:0xf\n\t" \
  "v_mov_b32 v33, v34\n\t" \
  "ds_read_b64_tr_b16 v[100:101], v141 offset:2048\n\t" \
  "ds_read_b64_tr_b16 v[102:103], v141 offset:2304\n\t" \
  "ds_read_b64_tr_b16 v[104:105], v141 offset:2560\n\t" \
  "ds_read_b64_tr_b16 v[106:107], v141 offset:2816\n\t" \
  "v_permlane32_swap_b32 v34, v33\n\t" \
  "v_max_f32 v34, v34, v33\n\t" \
  "v_mov_b32 v33, v34\n\t" \
  "ds_read_b64_tr_b16 v[108:109], v141 offset:3072\n\t" \
  "ds_read_b64_tr_b16 v[110:111], v141 offset:3328\n\t" \
  "ds_read_b64_tr_b16 v[112:113], v141 offset:3584\n\t" \
  "ds_read_b64_tr_b16 v[114:115], v141 offset:3840\n\t" \
  "ds_read_b128 v[12:15], v146 offset:11264\n\t" \
  "v_permlane16_swap_b32 v34, v33\n\t" \
  "v_max_f32 v34, v34, v33\n\t" \
  "v_mul_f32 v34, s81, v34\n\t" \
  "s_waitcnt lgkmcnt(7)\n\t" \
  "v_mfma_f32_16x16x32_bf16 v[124:127], v[100:103], v[84:87], 0\n\t" \
  "v_fma_f32 v120, v116, s81, -v34\n\t" \
  "v_fma_f32 v121, v117, s81, -v34\n\t" \
  "s_waitcnt lgkmcnt(5)\n\t" \
  "v_mfma_f32_16x16x32_bf16 v[124:127], v[104:107], v[88:91], v[124:127]\n\t" \
  "v_fma_f32 v122, v118, s81, -v34\n\t" \
  "v_fma_f32 v123, v119, s81, -v34\n\t" \
  "s_waitcnt lgkmcnt(3)\n\t" \
  "v_mfma_f32_16x16x32_bf16 v[124:127], v[108:111], v[92:95], v[124:127]\n\t" \
  "v_exp_f32 v120, v120\n\t" \
  "s_waitcnt lgkmcnt(1)\n\t" \
  "v_mfma_f32_16x16x32_bf16 v[124:127], v[112:115], v[96:99], v[124:127]\n\t" \
  "v_exp_f32 v121, v121\n\t" \
  "v_exp_f32 v122, v122\n\t" \
  "v_exp_f32 v123, v123\n\t" \
  "ds_read_b128 v[40:43], v143\n\t" \
  "v_cvt_pk_bf16_f32 v108, v120, v121\n\t" \
  "v_cvt_pk_bf16_f32 v109, v122, v123\n\t" \
  "v_mov_b32 v110, v108\n\t" \
  "v_mov_b32 v111, v109\n\t" \
  "v_mov_b32_dpp v108, v108 row_ror:8 row_mask:0xf bank_mask:0xc\n\t" \
  "v_mov_b32_dpp v109, v109 row_ror:8 row_mask:0xf bank_mask:0xc\n\t" \
  "v_mov_b32_dpp v110, v110 row_ror:8 row_mask:0xf bank_mask:0x3\n\t" \
  "v_mov_b32_dpp v111, v111 row_ror:8 row_mask:0xf bank_mask:0x3\n\t" \
  "v_mov_b32_dpp v124, v125 quad_perm:[0,1,2,3] row_mask:0xc bank_mask:0xf\n\t" \
  "v_mov_b32_dpp v126, v127 quad_perm:[0,1,2,3] row_mask:0xc bank_mask:0xf\n\t" \
  "v_mfma_f32_16x16x32_bf16 v[52:55], v[0:3], v[108:111], 0\n\t" \
  "ds_read_b128 v[44:47], v143 offset:16\n\t" \
  "v_mfma_f32_16x16x32_bf16 v[56:59], v[4:7], v[108:111], 0\n\t" \
  "s_waitcnt lgkmcnt(2)\n\t" \
  "v_mov_b32_dpp v124, v126 quad_perm:[0,1,2,3] row_mask:0xf bank_mask:0xc\n\t" \
  "v_fma_f32 v37, v124, s81, -v34\n\t" \
  "v_mfma_f32_16x16x32_bf16 v[60:63], v[8:11], v[108:111], 0\n\t" \
  "v_cmp_lt_f32 vcc, s79, v37\n\t" \
  "v_exp_f32 v37, v37\n\t" \
  "v_mfma_f32_16x16x32_bf16 v[64:67], v[12:15], v[108:111], 0\n\t" \
  "s_cbranch_vccnz .LH1bigm%=\n\t" \
  ".LH1bigr%=:\n\t" \
  "v_mov_b32 v38, v37\n\t" \
  "v_mfma_f32_16x16x32_bf16 v[76:79], v[108:111], v[154:157], 0\n\t" \
  "s_nop 0\n\t" \
  "v_mov_b32_dpp v37, v37 row_ror:8 row_mask:0xf bank_mask:0xc\n\t" \
  "v_mov_b32_dpp v38, v38 row_ror:8 row_mask:0xf bank_mask:0x3\n\t" \
  "v_cvt_pk_bf16_f32 v136, v37, v38\n\t" \
  "s_waitcnt lgkmcnt(0)\n\t" \
  "s_nop 0\n\t" \
  "v_mfma_f32_16x16x16_bf16 v[52:55], v[40:41], v[136:137], v[52:55]\n\t" \
  "s_mov_b64 exec, 1\n\t" \
  "buffer_load_dword v132, off, s[40:43], s75 sc1\n\t" \
  "s_mov_b64 exec, -1\n\t" \
  "v_mfma_f32_16x16x16_bf16 v[56:59], v[42:43], v[136:137], v[56:59]\n\t" \
  "v_mfma_f32_16x16x16_bf16 v[60:63], v[44:45], v[136:137], v[60:63]\n\t" \
  "v_mfma_f32_16x16x16_bf16 v[64:67], v[46:47], v[136:137], v[64:67]\n\t" \
  "v_cvt_pk_bf16_f32 v68, v52, v53\n\t" \
  "v_cvt_pk_bf16_f32 v69, v54, v55\n\t" \
  "v_mfma_f32_16x16x16_bf16 v[76:79], v[136:137], v[154:155], v[76:79]\n\t" \
  "v_cvt_pk_bf16_f32 v70, v56, v57\n\t" \
  "v_cvt_pk_bf16_f32 v71, v58, v59\n\t" \
  "v_cvt_pk_bf16_f32 v72, v60, v61\n\t" \
  "v_cvt_pk_bf16_f32 v73, v62, v63\n\t" \
  "v_mfma_f32_16x16x32_bf16 v[84:87], v[68:71], v[224:227], 0\n\t" \
  "v_cvt_pk_bf16_f32 v74, v64, v65\n\t" \
  "v_cvt_pk_bf16_f32 v75, v66, v67\n\t" \
  "v_mfma_f32_16x16x32_bf16 v[88:91], v[68:71], v[232:235], 0\n\t" \
  "s_add_u32 s68, s48, 65536\n\t" \
  "v_rcp_f32 v76, v76\n\t" \
  "v_mfma_f32_16x16x32_bf16 v[84:87], v[72:75], v[228:231], v[84:87]\n\t" \
  "v_rcp_f32 v77, v77\n\t" \
  "v_mfma_f32_16x16x32_bf16 v[88:91], v[72:75], v[236:239], v[88:91]\n\t" \
  "v_rcp_f32 v78, v78\n\t" \
  "v_mfma_f32_16x16x32_bf16 v[92:95], v[68:71], v[240:243], 0\n\t" \
  "v_rcp_f32 v79, v79\n\t" \
  "v_mfma_f32_16x16x32_bf16 v[96:99], v[68:71], v[248:251], 0\n\t" \
  "s_nop 0\n\t" \
  "v_mov_b32_dpp v84, v88 quad_perm:[0,1,2,3] row_mask:0xc bank_mask:0xf\n\t" \
  "v_mfma_f32_16x16x32_bf16 v[92:95], v[72:75], v[244:247], v[92:95]\n\t" \
  "v_mov_b32_dpp v85, v89 quad_perm:[0,1,2,3] row_mask:0xc bank_mask:0xf\n\t" \
  "v_mov_b32_dpp v86, v90 quad_perm:[0,1,2,3] row_mask:0xc bank_mask:0xf\n\t" \
  "v_mfma_f32_16x16x32_bf16 v[96:99], v[72:75], v[252:255], v[96:99]\n\t" \
  "v_mov_b32_dpp v87, v91 quad_perm:[0,1,2,3] row_mask:0xc bank_mask:0xf\n\t" \
  "v_mul_f32 v84, v84, v76\n\t" \
  "v_mul_f32 v85, v85, v77\n\t" \
  "v_mul_f32 v86, v86, v78\n\t" \
  "v_mul_f32 v87, v87, v79\n\t" \
  "v_cvt_pk_bf16_f32 v84, v84, v85\n\t" \
  "v_cvt_pk_bf16_f32 v85, v86, v87\n\t" \
  "buffer_store_dwordx2 v[84:85], v148, s[40:43], s48 offen sc1\n\t" \
  "v_mov_b32_dpp v92, v96 quad_perm:[0,1,2,3] row_mask:0xc bank_mask:0xf\n\t" \
  "v_mov_b32_dpp v93, v97 quad_perm:[0,1,2,3] row_mask:0xc bank_mask:0xf\n\t" \
  "v_mov_b32_dpp v94, v98 quad_perm:[0,1,2,3] row_mask:0xc bank_mask:0xf\n\t" \
  "v_mov_b32_dpp v95, v99 quad_perm:[0,1,2,3] row_mask:0xc bank_mask:0xf\n\t" \
  "v_mul_f32 v92, v92, v76\n\t" \
  "v_mul_f32 v93, v93, v77\n\t" \
  "v_mul_f32 v94, v94, v78\n\t" \
  "v_mul_f32 v95, v95, v79\n\t" \
  "v_cvt_pk_bf16_f32 v92, v92, v93\n\t" \
  "v_cvt_pk_bf16_f32 v93, v94, v95\n\t" \
  "buffer_store_dwordx2 v[92:93], v148, s[40:43], s68 offen sc1\n\t" \
  "s_memrealtime s[64:65]\n\t" \
  "ds_read_b64_tr_b16 v[8:9], v141 offset:2560\n\t" \
  "ds_read_b64_tr_b16 v[10:11], v141 offset:2816\n\t" \
  "ds_read_b128 v[120:123], v139 offset:15360\n\t" \
  "ds_read_b32 v129, v139 offset:45056\n\t" \
  "v_mov_b64 v[32:33], 0\n\t" \
  "v_mov_b64 v[34:35], 0\n\t" \
  "v_mov_b64 v[36:37], 0\n\t" \
  "v_mov_b64 v[38:39], 0\n\t" \
  "v_mov_b64 v[40:41], 0\n\t" \
  "v_mov_b64 v[42:43], 0\n\t" \
  "v_mov_b64 v[44:45], 0\n\t" \
  "v_mov_b64 v[46:47], 0\n\t" \
  "v_mov_b64 v[48:49], 0\n\t" \
  "v_mov_b64 v[50:51], 0\n\t" \
  "s_add_u32 s68, s48, s51\n\t" \
  "s_waitcnt vmcnt(2)\n\t" \
  "v_readfirstlane_b32 s70, v132\n\t" \
  "s_waitcnt lgkmcnt(0)\n\t" \
  "v_readfirstlane_b32 s72, v129\n\t" \
  "s_cmp_eq_u32 s61, 0\n\t" \
  "s_cselect_b32 s72, 0, s72\n\t" \
  "s_mov_b32 s92, s64\n\t" \
  "s_cmp_eq_u32 s70, 0\n\t" \
  "s_cbranch_scc1 .LH1tmwait%=\n\t" \
  ".LH1tmok%=:\n\t" \
  ".LH1w1%=:\n\t" \
  "s_memrealtime s[66:67]\n\t" \
  "s_waitcnt lgkmcnt(0)\n\t" \
  "s_sub_u32 s69, s66, s70\n\t" \
  "s_cmp_lt_i32 s69, 0\n\t" \
  "s_cbranch_scc1 .LH1w1%=\n\t" \
  "buffer_load_dwordx4 v[116:119], v139, s[40:43], s68 offen sc1\n\t" \
  "s_add_u32 s98, s70, s60\n\t" \
  "s_barrier\n\t" \
  "s_sub_u32 s69, s92, s91\n\t" \
  "s_mov_b32 s91, s98\n\t" \
  "s_sub_u32 s71, s61, s88\n\t" \
  "s_cmp_le_u32 s71, 3\n\t" \
  "s_cselect_b32 s71, 2, 0\n\t" \
  "s_min_u32 s69, s69, s76\n\t" \
  "s_cmp_lg_u32 s72, s61\n\t" \
  "s_cselect_b32 s73, s69, s73\n\t" \
  "s_cselect_b32 s76, s69, s76\n\t" \
  "s_cselect_b32 s88, s88, s61\n\t" \
  "s_cselect_b32 s71, 0, s71\n\t" \
  "s_add_u32 s93, s93, s71\n\t" \
  "buffer_load_dword v128, off, s[44:47], s56 sc1\n\t" \
  "s_add_u32 s69, s61, 1\n\t" \
  "s_and_b32 s69, s69, 3\n\t" \
  "s_lshl_b32 s69, s69, 16\n\t" \
  "s_add_u32 s70, s69, s50\n\t" \
  "s_waitcnt vmcnt(1)\n\t" \
  ".LH1redoa%=:\n\t" \
  "v_mfma_f32_16x16x32_bf16 v[124:127], v[116:119], v[120:123], 0\n\t" \
  "s_nop 7\n\t" \
  "v_permlane32_swap_b32 v124, v125\n\t" \
  "v_permlane32_swap_b32 v126, v127\n\t" \
  "v_add_f32 v124, v124, v125\n\t" \
  "v_add_f32 v126, v126, v127\n\t" \
  "s_nop 1\n\t" \
  "v_permlane16_swap_b32 v124, v126\n\t" \
  "v_add_f32 v124, v124, v126\n\t" \
  "ds_write_b32 v144, v124\n\t" \
  "v_cmp_u_f32 vcc, v124, v124\n\t" \
  "s_cbranch_vccnz .LH1faila%=\n\t" \
  ".LH1oka%=:\n\t" \
  "s_waitcnt lgkmcnt(0)\n\t" \
  "s_barrier\n\t" \
  "ds_read_b128 v[52:55], v147\n\t" \
  "ds_read_b128 v[56:59], v147 offset:1024\n\t" \
  "ds_read_b128 v[60:63], v147 offset:2048\n\t" \
  "ds_read_b128 v[64:67], v147 offset:3072\n\t" \
  "ds_read_b128 v[68:71], v147 offset:4096\n\t" \
  "ds_read_b128 v[72:75], v147 offset:5120\n\t" \
  "ds_read_b128 v[76:79], v147 offset:6144\n\t" \
  "ds_read_b128 v[80:83], v147 offset:7168\n\t" \
  "v_mov_b64 v[116:117], -1\n\t" \
  "v_mov_b64 v[118:119], -1\n\t" \
  "s_add_u32 s69, s61, 3\n\t" \
  "s_and_b32 s69, s69, 3\n\t" \
  "s_lshl_b32 s69, s69, 16\n\t" \
  "s_add_u32 s69, s69, s50\n\t" \
  "s_add_u32 s70, s70, s52\n\t" \
  ".LH1w2%=:\n\t" \
  "s_memrealtime s[66:67]\n\t" \
  "s_waitcnt lgkmcnt(0)\n\t" \
  "s_sub_u32 s71, s66, s98\n\t" \
  "s_cmp_lt_i32 s71, 0\n\t" \
  "s_cbranch_scc1 .LH1w2%=\n\t" \
  "buffer_load_dwordx4 v[0:3], v139, s[40:43], s70 offen sc1\n\t" \
  "buffer_load_dwordx4 v[4:7], v139, s[40:43], s70 offen offset:1024 sc1\n\t" \
  "s_barrier\n\t" \
  "buffer_store_dwordx4 v[116:119], v139, s[40:43], s68 offen sc1\n\t" \
  "buffer_store_dword v116, v149, s[40:43], s69 offen sc1\n\t" \
  "buffer_store_dword v130, v150, s[40:43], s57 offen sc1\n\t" \
  "buffer_store_dwordx4 v[8:11], v151, s[40:43], s58 offen sc1\n\t" \
  "s_mov_b32 s75, s90\n\t" \
  "s_add_u32 s90, s90, 128\n\t" \
  "s_add_u32 s68, s61, 4\n\t" \
  "s_lshr_b32 s68, s68, 1\n\t" \
  "s_cmp_ge_u32 s61, 62\n\t" \
  "s_cselect_b32 s68, 0, s68\n\t" \
  "s_waitcnt vmcnt(6)\n\t" \
  "v_readfirstlane_b32 s69, v128\n\t" \
  "s_cmp_lt_u32 s69, s68\n\t" \
  "s_cbranch_scc1 .LH1kvwait%=\n\t" \
  ".LH1kvok%=:\n\t" \
  "s_add_u32 m0, s77, 3072\n\t" \
  "s_nop 0\n\t" \
  "buffer_load_dwordx4 v139, s[40:43], s53 offen sc1 lds\n\t" \
  "s_add_u32 m0, s77, 4096\n\t" \
  "s_nop 0\n\t" \
  "buffer_load_dwordx4 v139, s[40:43], s54 offen sc1 lds\n\t" \
  "s_add_u32 m0, s77, 5120\n\t" \
  "s_nop 0\n\t" \
  "buffer_load_dwordx4 v139, s[40:43], s55 offen sc1 lds\n\t" \
  "s_xor_b32 s77, s77, 0xc000\n\t" \
  "s_xor_b32 s56, s56, 128\n\t" \
  "s_and_b32 s69, s61, 3\n\t" \
  "s_cmp_eq_u32 s69, 1\n\t" \
  "s_cselect_b32 s71, 0x2000, 0\n\t" \
  "s_add_u32 s53, s53, 0x80000\n\t" \
  "s_xor_b32 s53, s53, s71\n\t" \
  "s_add_u32 s54, s54, 0x80000\n\t" \
  "s_xor_b32 s54, s54, s71\n\t" \
  "s_add_u32 s55, s55, 0x80000\n\t" \
  "s_xor_b32 s55, s55, s71\n\t" \
  "s_cmp_eq_u32 s69, 3\n\t" \
  "s_cselect_b32 s71, 0x2000, 0\n\t" \
  "s_add_u32 s57, s57, 0x80000\n\t" \
  "s_xor_b32 s57, s57, s71\n\t" \
  "s_add_u32 s58, s58, 0x80000\n\t" \
  "s_xor_b32 s58, s58, s71\n\t" \
  "v_swap_b32 v146, v147\n\t" \
  "s_mov_b32 s71, s48\n\t" \
  "s_mov_b32 s48, s49\n\t" \
  "s_mov_b32 s49, s71\n\t" \
  "s_add_u32 s61, s61, 1\n\t" \
  "s_branch .LH1top%=\n\t" \
  ".LH1faila%=:\n\t" \
  "v_max3_u32 v129, v116, v117, v118\n\t" \
  "v_max_u32 v129, v129, v119\n\t" \
  "v_cmp_eq_u32 vcc, -1, v129\n\t" \
  "s_cbranch_vccz .LH1oka%=\n\t" \
  "s_add_u32 s83, s83, 1\n\t" \
  "s_add_u32 s71, s61, 1\n\t" \
  "v_mov_b32 v129, s71\n\t" \
  "ds_write_b32 v139, v129 offset:45056\n\t" \
  "s_cmp_lg_u32 s62, 0\n\t" \
  "s_cbranch_scc1 .LH1oka%=\n\t" \
  "s_add_u32 s63, s63, 1\n\t" \
  "s_and_b32 s71, s63, 255\n\t" \
  "s_cmp_lg_u32 s71, 0\n\t" \
  "s_cbranch_scc1 .LH1sga%=\n\t" \
  "buffer_load_dword v129, off, s[44:47], 0 sc1\n\t" \
  "s_waitcnt vmcnt(0)\n\t" \
  "v_readfirstlane_b32 s62, v129\n\t" \
  "s_cmp_lt_u32 s63, 0x100000\n\t" \
  "s_cbranch_scc1 .LH1sga%=\n\t" \
  "s_mov_b32 s62, 1\n\t" \
  "v_mov_b32 v129, 1\n\t" \
  "buffer_store_dword v129, off, s[44:47], 0 sc1\n\t" \
  ".LH1sga%=:\n\t" \
  "s_sleep 3\n\t" \
  "buffer_load_dwordx4 v[116:119], v139, s[40:43], s68 offen sc1\n\t" \
  "s_memrealtime s[66:67]\n\t" \
  "s_waitcnt lgkmcnt(0)\n\t" \
  "s_add_u32 s69, s66, s60\n\t" \
  "s_add_u32 s69, s69, s73\n\t" \
  "s_add_u32 s69, s69, s93\n\t" \
  "s_add_u32 s69, s69, s74\n\t" \
  "s_max_u32 s69, s69, 1\n\t" \
  "v_mov_b32 v132, s69\n\t" \
  "buffer_store_dword v132, v153, s[40:43], s90 offen sc1\n\t" \
  "s_waitcnt vmcnt(0)\n\t" \
  "s_branch .LH1redoa%=\n\t" \
  ".LH1failb%=:\n\t" \
  "s_add_u32 s84, s84, 1\n\t" \
  "v_mov_b32 v129, s61\n\t" \
  "ds_write_b32 v139, v129 offset:45056\n\t" \
  "s_cmp_lg_u32 s62, 0\n\t" \
  "s_cbranch_scc1 .LH1okb%=\n\t" \
  "s_add_u32 s63, s63, 1\n\t" \
  "s_and_b32 s71, s63, 255\n\t" \
  "s_cmp_lg_u32 s71, 0\n\t" \
  "s_cbranch_scc1 .LH1sgb%=\n\t" \
  "buffer_load_dword v129, off, s[44:47], 0 sc1\n\t" \
  "s_waitcnt vmcnt(0)\n\t" \
  "v_readfirstlane_b32 s62, v129\n\t" \
  "s_cmp_lt_u32 s63, 0x100000\n\t" \
  "s_cbranch_scc1 .LH1sgb%=\n\t" \
  "s_mov_b32 s62, 1\n\t" \
  "v_mov_b32 v129, 1\n\t" \
  "buffer_store_dword v129, off, s[44:47], 0 sc1\n\t" \
  ".LH1sgb%=:\n\t" \
  "s_sleep 3\n\t" \
  "buffer_load_dwordx4 v[0:3], v139, s[40:43], s70 offen sc1\n\t" \
  "buffer_load_dwordx4 v[4:7], v139, s[40:43], s70 offen offset:1024 sc1\n\t" \
  "s_memrealtime s[66:67]\n\t" \
  "s_waitcnt lgkmcnt(0)\n\t" \
  "s_add_u32 s69, s66, s73\n\t" \
  "s_add_u32 s69, s69, s93\n\t" \
  "s_add_u32 s69, s69, s74\n\t" \
  "s_max_u32 s69, s69, 1\n\t" \
  "v_mov_b32 v132, s69\n\t" \
  "buffer_store_dword v132, v153, s[40:43], s75 offen sc1\n\t" \
  "s_nop 8\n\t" \
  "v_mov_b64 v[32:33], 0\n\t" \
  "v_mov_b64 v[34:35], 0\n\t" \
  "v_mov_b64 v[36:37], 0\n\t" \
  "v_mov_b64 v[38:39], 0\n\t" \
  "v_mov_b64 v[40:41], 0\n\t" \
  "v_mov_b64 v[42:43], 0\n\t" \
  "v_mov_b64 v[44:45], 0\n\t" \
  "v_mov_b64 v[46:47], 0\n\t" \
  "s_waitcnt vmcnt(0)\n\t" \
  "s_branch .LH1redo%=\n\t" \
  ".LH1bigm%=:\n\t" \
  "v_mul_f32 v37, s81, v124\n\t" \
  "v_max_f32 v34, v34, v37\n\t" \
  "s_nop 1\n\t" \
  "v_max_f32_dpp v34, v34, v34 row_ror:8 row_mask:0xf bank_mask:0xf\n\t" \
  "v_mov_b32 v33, v34\n\t" \
  "s_nop 1\n\t" \
  "v_permlane32_swap_b32 v34, v33\n\t" \
  "v_max_f32 v34, v34, v33\n\t" \
  "v_mov_b32 v33, v34\n\t" \
  "s_nop 1\n\t" \
  "v_permlane16_swap_b32 v34, v33\n\t" \
  "v_max_f32 v34, v34, v33\n\t" \
  "v_fma_f32 v120, v116, s81, -v34\n\t" \
  "v_fma_f32 v121, v117, s81, -v34\n\t" \
  "v_fma_f32 v122, v118, s81, -v34\n\t" \
  "v_fma_f32 v123, v119, s81, -v34\n\t" \
  "v_fma_f32 v37, v124, s81, -v34\n\t" \
  "v_exp_f32 v120, v120\n\t" \
  "v_exp_f32 v121, v121\n\t" \
  "v_exp_f32 v122, v122\n\t" \
  "v_exp_f32 v123, v123\n\t" \
  "v_exp_f32 v37, v37\n\t" \
  "v_cvt_pk_bf16_f32 v108, v120, v121\n\t" \
  "v_cvt_pk_bf16_f32 v109, v122, v123\n\t" \
  "v_mov_b32 v110, v108\n\t" \
  "v_mov_b32 v111, v109\n\t" \
  "v_mov_b32_dpp v108, v108 row_ror:8 row_mask:0xf bank_mask:0xc\n\t" \
  "v_mov_b32_dpp v109, v109 row_ror:8 row_mask:0xf bank_mask:0xc\n\t" \
  "v_mov_b32_dpp v110, v110 row_ror:8 row_mask:0xf bank_mask:0x3\n\t" \
  "v_mov_b32_dpp v111, v111 row_ror:8 row_mask:0xf bank_mask:0x3\n\t" \
  "s_nop 7\n\t" \
  "v_mfma_f32_16x16x32_bf16 v[52:55], v[0:3], v[108:111], 0\n\t" \
  "v_mfma_f32_16x16x32_bf16 v[56:59], v[4:7], v[108:111], 0\n\t" \
  "v_mfma_f32_16x16x32_bf16 v[60:63], v[8:11], v[108:111], 0\n\t" \
  "v_mfma_f32_16x16x32_bf16 v[64:67], v[12:15], v[108:111], 0\n\t" \
  "s_branch .LH1bigr%=\n\t" \
  ".LH1kvwait%=:\n\t" \
  "buffer_load_dword v128, off, s[44:47], s56 sc1\n\t" \
  "s_waitcnt vmcnt(0)\n\t" \
  "v_readfirstlane_b32 s69, v128\n\t" \
  "s_cmp_ge_u32 s69, s68\n\t" \
  "s_cbranch_scc1 .LH1kvok%=\n\t" \
  "s_sleep 4\n\t" \
  "s_cmp_lg_u32 s62, 0\n\t" \
  "s_cbranch_scc1 .LH1kvok%=\n\t" \
  "s_add_u32 s63, s63, 1\n\t" \
  "s_and_b32 s71, s63, 255\n\t" \
  "s_cmp_lg_u32 s71, 0\n\t" \
  "s_cbranch_scc1 .LH1sgk%=\n\t" \
  "buffer_load_dword v129, off, s[44:47], 0 sc1\n\t" \
  "s_waitcnt vmcnt(0)\n\t" \
  "v_readfirstlane_b32 s62, v129\n\t" \
  "s_cmp_lt_u32 s63, 0x100000\n\t" \
  "s_cbranch_scc1 .LH1sgk%=\n\t" \
  "s_mov_b32 s62, 1\n\t" \
  "v_mov_b32 v129, 1\n\t" \
  "buffer_store_dword v129, off, s[44:47], 0 sc1\n\t" \
  ".LH1sgk%=:\n\t" \
  "s_branch .LH1kvwait%=\n\t" \
  ".LH1tmwait%=:\n\t" \
  "buffer_load_dword v132, off, s[40:43], s75 sc1\n\t" \
  "s_waitcnt vmcnt(0)\n\t" \
  "v_readfirstlane_b32 s70, v132\n\t" \
  "s_cmp_lg_u32 s70, 0\n\t" \
  "s_cbranch_scc1 .LH1tmok%=\n\t" \
  "s_mov_b32 s70, s92\n\t" \
  "s_cmp_lg_u32 s62, 0\n\t" \
  "s_cbranch_scc1 .LH1tmok%=\n\t" \
  "s_add_u32 s63, s63, 1\n\t" \
  "s_and_b32 s71, s63, 255\n\t" \
  "s_cmp_lg_u32 s71, 0\n\t" \
  "s_cbranch_scc1 .LH1sgt%=\n\t" \
  "buffer_load_dword v129, off, s[44:47], 0 sc1\n\t" \
  "s_waitcnt vmcnt(0)\n\t" \
  "v_readfirstlane_b32 s62, v129\n\t" \
  "s_cmp_lt_u32 s63, 0x100000\n\t" \
  "s_cbranch_scc1 .LH1sgt%=\n\t" \
  "s_mov_b32 s62, 1\n\t" \
  "v_mov_b32 v129, 1\n\t" \
  "buffer_store_dword v129, off, s[44:47], 0 sc1\n\t" \
  ".LH1sgt%=:\n\t" \
  "s_branch .LH1tmwait%=\n\t" \
  ".LH1last%=:\n\t" \
  "ds_read_b64_tr_b16 v[8:9], v141 offset:2560\n\t" \
  "ds_read_b64_tr_b16 v[10:11], v141 offset:2816\n\t" \
  "s_waitcnt lgkmcnt(0)\n\t" \
  "buffer_store_dword v130, v150, s[40:43], s57 offen sc1\n\t" \
  "buffer_store_dwordx4 v[8:11], v151, s[40:43], s58 offen sc1\n\t" \
  "s_waitcnt vmcnt(0)\n\t" \
  "s_barrier\n\t" \
  "s_branch .LHend%=\n\t" \
  ".LH2start%=:\n\t" \
  "global_load_dword v0, v[128:129], off\n\t" \
  "global_load_dword v1, v[128:129], off offset:8\n\t" \
  "global_load_dword v2, v[128:129], off offset:16\n\t" \
  "global_load_dword v3, v[128:129], off offset:24\n\t" \
  "global_load_dword v4, v[128:129], off offset:128\n\t" \
  "global_load_dword v5, v[128:129], off offset:136\n\t" \
  "global_load_dword v6, v[128:129], off offset:144\n\t" \
  "global_load_dword v7, v[128:129], off offset:152\n\t" \
  "ds_read_b128 v[52:55], v146\n\t" \
  "ds_read_b128 v[56:59], v146 offset:1024\n\t" \
  "ds_read_b128 v[60:63], v146 offset:2048\n\t" \
  "ds_read_b128 v[64:67], v146 offset:3072\n\t" \
  "ds_read_b128 v[68:71], v146 offset:4096\n\t" \
  "ds_read_b128 v[72:75], v146 offset:5120\n\t" \
  "ds_read_b128 v[76:79], v146 offset:6144\n\t" \
  "ds_read_b128 v[80:83], v146 offset:7168\n\t" \
  "v_mov_b64 v[32:33], 0\n\t" \
  "v_mov_b64 v[34:35], 0\n\t" \
  "v_mov_b64 v[36:37], 0\n\t" \
  "v_mov_b64 v[38:39], 0\n\t" \
  "v_mov_b64 v[40:41], 0\n\t" \
  "v_mov_b64 v[42:43], 0\n\t" \
  "v_mov_b64 v[44:45], 0\n\t" \
  "v_mov_b64 v[46:47], 0\n\t" \
  "v_mov_b64 v[48:49], 0\n\t" \
  "v_mov_b64 v[50:51], 0\n\t" \
  "v_mov_b32 v131, 0\n\t" \
  "v_mov_b32 v137, 0\n\t" \
  "v_mov_b32 v154, 0x3f803f80\n\t" \
  "v_mov_b32 v155, 0x3f803f80\n\t" \
  "v_mov_b32 v156, 0x3f803f80\n\t" \
  "v_mov_b32 v157, 0x3f803f80\n\t" \
  "s_waitcnt vmcnt(0)\n\t" \
  "v_mov_b32 v128, 0\n\t" \
  "s_memrealtime s[66:67]\n\t" \
  "s_waitcnt lgkmcnt(0)\n\t" \
  "s_mov_b32 s91, s66\n\t" \
  "s_add_u32 s66, s66, s88\n\t" \
  "s_or_b32 s66, s66, 1\n\t" \
  "v_mov_b32 v132, s66\n\t" \
  "s_mov_b32 s88, -8\n\t" \
  "s_mov_b32 s77, 16384\n\t" \
  "s_mov_b32 s75, s90\n\t" \
  "s_add_u32 s90, s90, 128\n\t" \
  "s_mov_b32 s74, s89\n\t" \
  "s_nop 0\n\t" \
  "s_mov_b32 s73, s59\n\t" \
  "s_add_u32 s76, s76, s59\n\t" \
  "v_min_u32 v0, -2, v0\n\t" \
  "v_min_u32 v1, -2, v1\n\t" \
  "v_min_u32 v2, -2, v2\n\t" \
  "v_min_u32 v3, -2, v3\n\t" \
  "v_min_u32 v4, -2, v4\n\t" \
  "v_min_u32 v5, -2, v5\n\t" \
  "v_min_u32 v6, -2, v6\n\t" \
  "v_min_u32 v7, -2, v7\n\t" \
  "s_nop 1\n\t" \
  ".LH2top%=:\n\t" \
  "s_waitcnt vmcnt(6)\n\t" \
  ".LH2redo%=:\n\t" \
  "ds_write_b128 v139, v[0:3] offset:4096\n\t" \
  "v_smfmac_f32_16x16x64_bf16 v[32:35], v[0:3], a[32:39], v138\n\t" \
  "ds_write_b128 v139, v[4:7] offset:5120\n\t" \
  "v_smfmac_f32_16x16x64_bf16 v[36:39], v[0:3], a[96:103], v138\n\t" \
  "v_max3_u32 v129, v0, v1, v2\n\t" \
  "v_max3_u32 v129, v129, v3, v4\n\t" \
  "v_smfmac_f32_16x16x64_bf16 v[40:43], v[0:3], a[160:167], v138\n\t" \
  "v_max3_u32 v129, v129, v5, v6\n\t" \
  "v_max_u32 v129, v129, v7\n\t" \
  "v_smfmac_f32_16x16x64_bf16 v[44:47], v[0:3], a[224:231], v138\n\t" \
  "v_cmp_eq_u32 vcc, -1, v129\n\t" \
  "s_cbranch_vccnz .LH2failb%=\n\t" \
  ".LH2okb%=:\n\t" \
  "s_waitcnt lgkmcnt(0)\n\t" \
  "s_barrier\n\t" \
  "ds_read_b128 v[8:11], v139 offset:6144\n\t" \
  "v_smfmac_f32_16x16x64_bf16 v[32:35], v[4:7], a[40:47], v138\n\t" \
  "ds_read_b128 v[12:15], v139 offset:7168\n\t" \
  "v_smfmac_f32_16x16x64_bf16 v[36:39], v[4:7], a[104:111], v138\n\t" \
  "ds_read_b128 v[16:19], v139\n\t" \
  "v_smfmac_f32_16x16x64_bf16 v[40:43], v[4:7], a[168:175], v138\n\t" \
  "ds_read_b128 v[20:23], v139 offset:1024\n\t" \
  "v_smfmac_f32_16x16x64_bf16 v[44:47], v[4:7], a[232:239], v138\n\t" \
  "ds_read_b128 v[24:27], v139 offset:2048\n\t" \
  "v_smfmac_f32_16x16x64_bf16 v[48:51], v[0:3], v[192:199], v138\n\t" \
  "ds_read_b128 v[28:31], v139 offset:3072\n\t" \
  "v_smfmac_f32_16x16x64_bf16 v[48:51], v[4:7], v[200:207], v138\n\t" \
  "s_waitcnt lgkmcnt(5)\n\t" \
  "v_smfmac_f32_16x16x64_bf16 v[32:35], v[8:11], a[48:55], v138\n\t" \
  "v_smfmac_f32_16x16x64_bf16 v[36:39], v[8:11], a[112:119], v138\n\t" \
  "s_waitcnt lgkmcnt(4)\n\t" \
  "v_smfmac_f32_16x16x64_bf16 v[32:35], v[12:15], a[56:63], v138\n\t" \
  "v_smfmac_f32_16x16x64_bf16 v[36:39], v[12:15], a[120:127], v138\n\t" \
  "s_waitcnt lgkmcnt(3)\n\t" \
  "v_smfmac_f32_16x16x64_bf16 v[32:35], v[16:19], a[0:7], v138\n\t" \
  "v_smfmac_f32_16x16x64_bf16 v[36:39], v[16:19], a[64:71], v138\n\t" \
  "s_waitcnt lgkmcnt(2)\n\t" \
  "v_smfmac_f32_16x16x64_bf16 v[32:35], v[20:23], a[8:15], v138\n\t" \
  "v_smfmac_f32_16x16x64_bf16 v[36:39], v[20:23], a[72:79], v138\n\t" \
  "s_waitcnt lgkmcnt(1)\n\t" \
  "v_smfmac_f32_16x16x64_bf16 v[32:35], v[24:27], a[16:23], v138\n\t" \
  "ds_read_b128 v[0:3], v146 offset:8192\n\t" \
  "v_smfmac_f32_16x16x64_bf16 v[36:39], v[24:27], a[80:87], v138\n\t" \
  "s_waitcnt lgkmcnt(1)\n\t" \
  "v_smfmac_f32_16x16x64_bf16 v[32:35], v[28:31], a[24:31], v138\n\t" \
  "ds_read_b128 v[4:7], v146 offset:9216\n\t" \
  "v_smfmac_f32_16x16x64_bf16 v[36:39], v[28:31], a[88:95], v138\n\t" \
  "v_smfmac_f32_16x16x64_bf16 v[48:51], v[8:11], v[208:215], v138\n\t" \
  "v_smfmac_f32_16x16x64_bf16 v[48:51], v[12:15], v[216:223], v138\n\t" \
  "s_nop 0\n\t" \
  "v_permlane32_swap_b32 v32, v36\n\t" \
  "v_smfmac_f32_16x16x64_bf16 v[48:51], v[16:19], v[160:167], v138\n\t" \
  "v_permlane32_swap_b32 v33, v37\n\t" \
  "v_smfmac_f32_16x16x64_bf16 v[48:51], v[20:23], v[168:175], v138\n\t" \
  "v_permlane32_swap_b32 v34, v38\n\t" \
  "v_smfmac_f32_16x16x64_bf16 v[48:51], v[24:27], v[176:183], v138\n\t" \
  "v_permlane32_swap_b32 v35, v39\n\t" \
  "v_smfmac_f32_16x16x64_bf16 v[48:51], v[28:31], v[184:191], v138\n\t" \
  "v_add_f32 v32, v32, v36\n\t" \
  "v_add_f32 v33, v33, v37\n\t" \
  "v_smfmac_f32_16x16x64_bf16 v[40:43], v[8:11], a[176:183], v138\n\t" \
  "v_add_f32 v34, v34, v38\n\t" \
  "v_add_f32 v35, v35, v39\n\t" \
  "v_smfmac_f32_16x16x64_bf16 v[44:47], v[8:11], a[240:247], v138\n\t" \
  "v_cvt_pk_bf16_f32 v32, v32, v33\n\t" \
  "v_cvt_pk_bf16_f32 v33, v34, v35\n\t" \
  "v_smfmac_f32_16x16x64_bf16 v[40:43], v[12:15], a[184:191], v138\n\t" \
  "ds_write_b64 v140, v[32:33]\n\t" \
  "v_smfmac_f32_16x16x64_bf16 v[44:47], v[12:15], a[248:255], v138\n\t" \
  "v_permlane32_swap_b32 v48, v49\n\t" \
  "v_smfmac_f32_16x16x64_bf16 v[40:43], v[16:19], a[128:135], v138\n\t" \
  "v_permlane32_swap_b32 v50, v51\n\t" \
  "v_smfmac_f32_16x16x64_bf16 v[44:47], v[16:19], a[192:199], v138\n\t" \
  "v_add_f32 v48, v48, v49\n\t" \
  "v_add_f32 v50, v50, v51\n\t" \
  "v_smfmac_f32_16x16x64_bf16 v[40:43], v[20:23], a[136:143], v138\n\t" \
  "v_cvt_pk_bf16_f32 v130, v48, v50\n\t" \
  "v_smfmac_f32_16x16x64_bf16 v[44:47], v[20:23], a[200:207], v138\n\t" \
  "s_waitcnt lgkmcnt(0)\n\t" \
  "s_barrier\n\t" \
  "ds_read_b64_tr_b16 v[84:85], v141\n\t" \
  "ds_read_b64_tr_b16 v[86:87], v141 offset:256\n\t" \
  "v_smfmac_f32_16x16x64_bf16 v[40:43], v[24:27], a[144:151], v138\n\t" \
  "ds_read_b64_tr_b16 v[88:89], v141 offset:512\n\t" \
  "ds_read_b64_tr_b16 v[90:91], v141 offset:768\n\t" \
  "v_smfmac_f32_16x16x64_bf16 v[44:47], v[24:27], a[208:215], v138\n\t" \
  "ds_read_b64_tr_b16 v[92:93], v141 offset:1024\n\t" \
  "ds_read_b64_tr_b16 v[94:95], v141 offset:1280\n\t" \
  "v_smfmac_f32_16x16x64_bf16 v[40:43], v[28:31], a[152:159], v138\n\t" \
  "ds_read_b64_tr_b16 v[96:97], v141 offset:1536\n\t" \
  "ds_read_b64_tr_b16 v[98:99], v141 offset:1792\n\t" \
  "v_smfmac_f32_16x16x64_bf16 v[44:47], v[28:31], a[216:223], v138\n\t" \
  "ds_write_b64 v142, v[130:131]\n\t" \
  "s_waitcnt lgkmcnt(7)\n\t" \
  "v_mfma_f32_16x16x32_bf16 v[116:119], v[52:55], v[84:87], 0\n\t" \
  "ds_read_b128 v[8:11], v146 offset:10240\n\t" \
  "v_mfma_f32_16x16x32_bf16 v[120:123], v[68:71], v[84:87], 0\n\t" \
  "v_permlane32_swap_b32 v40, v44\n\t" \
  "v_permlane32_swap_b32 v41, v45\n\t" \
  "s_waitcnt lgkmcnt(6)\n\t" \
  "v_mfma_f32_16x16x32_bf16 v[116:119], v[56:59], v[88:91], v[116:119]\n\t" \
  "v_permlane32_swap_b32 v42, v46\n\t" \
  "v_permlane32_swap_b32 v43, v47\n\t" \
  "v_mfma_f32_16x16x32_bf16 v[120:123], v[72:75], v[88:91], v[120:123]\n\t" \
  "v_add_f32 v40, v40, v44\n\t" \
  "v_add_f32 v41, v41, v45\n\t" \
  "s_waitcnt lgkmcnt(4)\n\t" \
  "v_mfma_f32_16x16x32_bf16 v[116:119], v[60:63], v[92:95], v[116:119]\n\t" \
  "v_add_f32 v42, v42, v46\n\t" \
  "v_add_f32 v43, v43, v47\n\t" \
  "v_mfma_f32_16x16x32_bf16 v[120:123], v[76:79], v[92:95], v[120:123]\n\t" \
  "v_cvt_pk_bf16_f32 v40, v40, v41\n\t" \
  "v_cvt_pk_bf16_f32 v41, v42, v43\n\t" \
  "s_waitcnt lgkmcnt(2)\n\t" \
  "v_mfma_f32_16x16x32_bf16 v[116:119], v[64:67], v[96:99], v[116:119]\n\t" \
  "ds_write_b64 v140, v[40:41] offset:2048\n\t" \
  "v_mfma_f32_16x16x32_bf16 v[120:123], v[80:83], v[96:99], v[120:123]\n\t" \
  "s_nop 7\n\t" \
  "v_mov_b32_dpp v116, v120 quad_perm:[0,1,2,3] row_mask:0xf bank_mask:0xc\n\t" \
  "v_mov_b32_dpp v117, v121 quad_perm:[0,1,2,3] row_mask:0xf bank_mask:0xc\n\t" \
  "v_mov_b32_dpp v118, v122 quad_perm:[0,1,2,3] row_mask:0xf bank_mask:0xc\n\t" \
  "v_mov_b32_dpp v119, v123 quad_perm:[0,1,2,3] row_mask:0xf bank_mask:0xc\n\t" \
  "v_max3_f32 v34, v116, v117, v118\n\t" \
  "v_max_f32 v34, v34, v119\n\t" \
  "s_cmp_eq_u32 s61, 63\n\t" \
  "s_waitcnt lgkmcnt(0)\n\t" \
  "s_barrier\n\t" \
  "s_cbranch_scc1 .LH2last%=\n\t" \
  "v_max_f32_dpp v34, v34, v34 row_ror:8 row_mask:0xf bank_mask:0xf\n\t" \
  "v_mov_b32 v33, v34\n\t" \
  "ds_read_b64_tr_b16 v[100:101], v141 offset:2048\n\t" \
  "ds_read_b64_tr_b16 v[102:103], v141 offset:2304\n\t" \
  "ds_read_b64_tr_b16 v[104:105], v141 offset:2560\n\t" \
  "ds_read_b64_tr_b16 v[106:107], v141 offset:2816\n\t" \
  "v_permlane32_swap_b32 v34, v33\n\t" \
  "v_max_f32 v34, v34, v33\n\t" \
  "v_mov_b32 v33, v34\n\t" \
  "ds_read_b64_tr_b16 v[108:109], v141 offset:3072\n\t" \
  "ds_read_b64_tr_b16 v[110:111], v141 offset:3328\n\t" \
  "ds_read_b64_tr_b16 v[112:113], v141 offset:3584\n\t" \
  "ds_read_b64_tr_b16 v[114:115], v141 offset:3840\n\t" \
  "ds_read_b128 v[12:15], v146 offset:11264\n\t" \
  "v_permlane16_swap_b32 v34, v33\n\t" \
  "v_max_f32 v34, v34, v33\n\t" \
  "v_mul_f32 v34, s81, v34\n\t" \
  "s_waitcnt lgkmcnt(7)\n\t" \
  "v_mfma_f32_16x16x32_bf16 v[124:127], v[100:103], v[84:87], 0\n\t" \
  "v_fma_f32 v120, v116, s81, -v34\n\t" \
  "v_fma_f32 v121, v117, s81, -v34\n\t" \
  "s_waitcnt lgkmcnt(5)\n\t" \
  "v_mfma_f32_16x16x32_bf16 v[124:127], v[104:107], v[88:91], v[124:127]\n\t" \
  "v_fma_f32 v122, v118, s81, -v34\n\t" \
  "v_fma_f32 v123, v119, s81, -v34\n\t" \
  "s_waitcnt lgkmcnt(3)\n\t" \
  "v_mfma_f32_16x16x32_bf16 v[124:127], v[108:111], v[92:95], v[124:127]\n\t" \
  "v_exp_f32 v120, v120\n\t" \
  "s_waitcnt lgkmcnt(1)\n\t" \
  "v_mfma_f32_16x16x32_bf16 v[124:127], v[112:115], v[96:99], v[124:127]\n\t" \
  "v_exp_f32 v121, v121\n\t" \
  "v_exp_f32 v122, v122\n\t" \
  "v_exp_f32 v123, v123\n\t" \
  "ds_read_b128 v[40:43], v143\n\t" \
  "v_cvt_pk_bf16_f32 v108, v120, v121\n\t" \
  "v_cvt_pk_bf16_f32 v109, v122, v123\n\t" \
  "v_mov_b32 v110, v108\n\t" \
  "v_mov_b32 v111, v109\n\t" \
  "v_mov_b32_dpp v108, v108 row_ror:8 row_mask:0xf bank_mask:0xc\n\t" \
  "v_mov_b32_dpp v109, v109 row_ror:8 row_mask:0xf bank_mask:0xc\n\t" \
  "v_mov_b32_dpp v110, v110 row_ror:8 row_mask:0xf bank_mask:0x3\n\t" \
  "v_mov_b32_dpp v111, v111 row_ror:8 row_mask:0xf bank_mask:0x3\n\t" \
  "v_mov_b32_dpp v124, v125 quad_perm:[0,1,2,3] row_mask:0xc bank_mask:0xf\n\t" \
  "v_mov_b32_dpp v126, v127 quad_perm:[0,1,2,3] row_mask:0xc bank_mask:0xf\n\t" \
  "v_mfma_f32_16x16x32_bf16 v[52:55], v[0:3], v[108:111], 0\n\t" \
  "ds_read_b128 v[44:47], v143 offset:16\n\t" \
  "v_mfma_f32_16x16x32_bf16 v[56:59], v[4:7], v[108:111], 0\n\t" \
  "s_waitcnt lgkmcnt(2)\n\t" \
  "v_mov_b32_dpp v124, v126 quad_perm:[0,1,2,3] row_mask:0xf bank_mask:0xc\n\t" \
  "v_fma_f32 v37, v124, s81, -v34\n\t" \
  "v_mfma_f32_16x16x32_bf16 v[60:63], v[8:11], v[108:111], 0\n\t" \
  "v_cmp_lt_f32 vcc, s79, v37\n\t" \
  "v_exp_f32 v37, v37\n\t" \
  "v_mfma_f32_16x16x32_bf16 v[64:67], v[12:15], v[108:111], 0\n\t" \
  "s_cbranch_vccnz .LH2bigm%=\n\t" \
  ".LH2bigr%=:\n\t" \
  "v_mov_b32 v38, v37\n\t" \
  "v_mfma_f32_16x16x32_bf16 v[76:79], v[108:111], v[154:157], 0\n\t" \
  "s_nop 0\n\t" \
  "v_mov_b32_dpp v37, v37 row_ror:8 row_mask:0xf bank_mask:0xc\n\t" \
  "v_mov_b32_dpp v38, v38 row_ror:8 row_mask:0xf bank_mask:0x3\n\t" \
  "v_cvt_pk_bf16_f32 v136, v37, v38\n\t" \
  "s_waitcnt lgkmcnt(0)\n\t" \
  "s_nop 0\n\t" \
  "v_mfma_f32_16x16x16_bf16 v[52:55], v[40:41], v[136:137], v[52:55]\n\t" \
  "s_mov_b64 exec, 1\n\t" \
  "buffer_load_dword v132, off, s[40:43], s75 sc1\n\t" \
  "s_mov_b64 exec, -1\n\t" \
  "v_mfma_f32_16x16x16_bf16 v[56:59], v[42:43], v[136:137], v[56:59]\n\t" \
  "v_mfma_f32_16x16x16_bf16 v[60:63], v[44:45], v[136:137], v[60:63]\n\t" \
  "v_mfma_f32_16x16x16_bf16 v[64:67], v[46:47], v[136:137], v[64:67]\n\t" \
  "v_cvt_pk_bf16_f32 v68, v52, v53\n\t" \
  "v_cvt_pk_bf16_f32 v69, v54, v55\n\t" \
  "v_mfma_f32_16x16x16_bf16 v[76:79], v[136:137], v[154:155], v[76:79]\n\t" \
  "v_cvt_pk_bf16_f32 v70, v56, v57\n\t" \
  "v_cvt_pk_bf16_f32 v71, v58, v59\n\t" \
  "v_cvt_pk_bf16_f32 v72, v60, v61\n\t" \
  "v_cvt_pk_bf16_f32 v73, v62, v63\n\t" \
  "v_mfma_f32_16x16x32_bf16 v[84:87], v[68:71], v[224:227], 0\n\t" \
  "v_cvt_pk_bf16_f32 v74, v64, v65\n\t" \
  "v_cvt_pk_bf16_f32 v75, v66, v67\n\t" \
  "v_mfma_f32_16x16x32_bf16 v[88:91], v[68:71], v[232:235], 0\n\t" \
  "s_add_u32 s68, s48, 65536\n\t" \
  "v_rcp_f32 v76, v76\n\t" \
  "v_mfma_f32_16x16x32_bf16 v[84:87], v[72:75], v[228:231], v[84:87]\n\t" \
  "v_rcp_f32 v77, v77\n\t" \
  "v_mfma_f32_16x16x32_bf16 v[88:91], v[72:75], v[236:239], v[88:91]\n\t" \
  "v_rcp_f32 v78, v78\n\t" \
  "v_mfma_f32_16x16x32_bf16 v[92:95], v[68:71], v[240:243], 0\n\t" \
  "v_rcp_f32 v79, v79\n\t" \
  "v_mfma_f32_16x16x32_bf16 v[96:99], v[68:71], v[248:251], 0\n\t" \
  "s_nop 0\n\t" \
  "v_mov_b32_dpp v84, v88 quad_perm:[0,1,2,3] row_mask:0xc bank_mask:0xf\n\t" \
  "v_mfma_f32_16x16x32_bf16 v[92:95], v[72:75], v[244:247], v[92:95]\n\t" \
  "v_mov_b32_dpp v85, v89 quad_perm:[0,1,2,3] row_mask:0xc bank_mask:0xf\n\t" \
  "v_mov_b32_dpp v86, v90 quad_perm:[0,1,2,3] row_mask:0xc bank_mask:0xf\n\t" \
  "v_mfma_f32_16x16x32_bf16 v[96:99], v[72:75], v[252:255], v[96:99]\n\t" \
  "v_mov_b32_dpp v87, v91 quad_perm:[0,1,2,3] row_mask:0xc bank_mask:0xf\n\t" \
  "v_mul_f32 v84, v84, v76\n\t" \
  "v_mul_f32 v85, v85, v77\n\t" \
  "v_mul_f32 v86, v86, v78\n\t" \
  "v_mul_f32 v87, v87, v79\n\t" \
  "v_cvt_pk_bf16_f32 v84, v84, v85\n\t" \
  "v_cvt_pk_bf16_f32 v85, v86, v87\n\t" \
  "buffer_store_dwordx2 v[84:85], v148, s[40:43], s48 offen sc1\n\t" \
  "v_mov_b32_dpp v92, v96 quad_perm:[0,1,2,3] row_mask:0xc bank_mask:0xf\n\t" \
  "v_mov_b32_dpp v93, v97 quad_perm:[0,1,2,3] row_mask:0xc bank_mask:0xf\n\t" \
  "v_mov_b32_dpp v94, v98 quad_perm:[0,1,2,3] row_mask:0xc bank_mask:0xf\n\t" \
  "v_mov_b32_dpp v95, v99 quad_perm:[0,1,2,3] row_mask:0xc bank_mask:0xf\n\t" \
  "v_mul_f32 v92, v92, v76\n\t" \
  "v_mul_f32 v93, v93, v77\n\t" \
  "v_mul_f32 v94, v94, v78\n\t" \
  "v_mul_f32 v95, v95, v79\n\t" \
  "v_cvt_pk_bf16_f32 v92, v92, v93\n\t" \
  "v_cvt_pk_bf16_f32 v93, v94, v95\n\t" \
  "buffer_store_dwordx2 v[92:93], v148, s[40:43], s68 offen sc1\n\t" \
  "s_memrealtime s[64:65]\n\t" \
  "ds_read_b64_tr_b16 v[8:9], v141 offset:3072\n\t" \
  "ds_read_b64_tr_b16 v[10:11], v141 offset:3328\n\t" \
  "ds_read_b128 v[120:123], v139 offset:15360\n\t" \
  "ds_read_b32 v129, v139 offset:45056\n\t" \
  "v_mov_b64 v[32:33], 0\n\t" \
  "v_mov_b64 v[34:35], 0\n\t" \
  "v_mov_b64 v[36:37], 0\n\t" \
  "v_mov_b64 v[38:39], 0\n\t" \
  "v_mov_b64 v[40:41], 0\n\t" \
  "v_mov_b64 v[42:43], 0\n\t" \
  "v_mov_b64 v[44:45], 0\n\t" \
  "v_mov_b64 v[46:47], 0\n\t" \
  "v_mov_b64 v[48:49], 0\n\t" \
  "v_mov_b64 v[50:51], 0\n\t" \
  "s_add_u32 s68, s48, s51\n\t" \
  "s_waitcnt vmcnt(2)\n\t" \
  "v_readfirstlane_b32 s70, v132\n\t" \
  "s_waitcnt lgkmcnt(0)\n\t" \
  "v_readfirstlane_b32 s72, v129\n\t" \
  "s_cmp_eq_u32 s61, 0\n\t" \
  "s_cselect_b32 s72, 0, s72\n\t" \
  "s_mov_b32 s92, s64\n\t" \
  "s_cmp_eq_u32 s70, 0\n\t" \
  "s_cbranch_scc1 .LH2tmwait%=\n\t" \
  ".LH2tmok%=:\n\t" \
  ".LH2w1%=:\n\t" \
  "s_memrealtime s[66:67]\n\t" \
  "s_waitcnt lgkmcnt(0)\n\t" \
  "s_sub_u32 s69, s66, s70\n\t" \
  "s_cmp_lt_i32 s69, 0\n\t" \
  "s_cbranch_scc1 .LH2w1%=\n\t" \
  "buffer_load_dwordx4 v[116:119], v139, s[40:43], s68 offen sc1\n\t" \
  "s_add_u32 s98, s70, s60\n\t" \
  "s_barrier\n\t" \
  "s_sub_u32 s69, s92, s91\n\t" \
  "s_mov_b32 s91, s98\n\t" \
  "s_sub_u32 s71, s61, s88\n\t" \
  "s_cmp_le_u32 s71, 3\n\t" \
  "s_cselect_b32 s71, 2, 0\n\t" \
  "s_min_u32 s69, s69, s76\n\t" \
  "s_cmp_lg_u32 s72, s61\n\t" \
  "s_cselect_b32 s73, s69, s73\n\t" \
  "s_cselect_b32 s76, s69, s76\n\t" \
  "s_cselect_b32 s88, s88, s61\n\t" \
  "s_cselect_b32 s71, 0, s71\n\t" \
  "s_add_u32 s93, s93, s71\n\t" \
  "v_mov_b32 v129, s61\n\t" \
  "buffer_store_dword v129, v152, s[44:47], 0 offen sc1\n\t" \
  "buffer_load_dword v128, off, s[44:47], s56 sc1\n\t" \
  "s_add_u32 s69, s61, 1\n\t" \
  "s_and_b32 s69, s69, 3\n\t" \
  "s_lshl_b32 s69, s69, 16\n\t" \
  "s_add_u32 s70, s69, s50\n\t" \
  "s_waitcnt vmcnt(2)\n\t" \
  ".LH2redoa%=:\n\t" \
  "v_mfma_f32_16x16x32_bf16 v[124:127], v[116:119], v[120:123], 0\n\t" \
  "s_nop 7\n\t" \
  "v_permlane32_swap_b32 v124, v125\n\t" \
  "v_permlane32_swap_b32 v126, v127\n\t" \
  "v_add_f32 v124, v124, v125\n\t" \
  "v_add_f32 v126, v126, v127\n\t" \
  "s_nop 1\n\t" \
  "v_permlane16_swap_b32 v124, v126\n\t" \
  "v_add_f32 v124, v124, v126\n\t" \
  "ds_write_b32 v144, v124\n\t" \
  "v_cmp_u_f32 vcc, v124, v124\n\t" \
  "s_cbranch_vccnz .LH2faila%=\n\t" \
  ".LH2oka%=:\n\t" \
  "s_waitcnt lgkmcnt(0)\n\t" \
  "s_barrier\n\t" \
  "ds_read_b128 v[52:55], v147\n\t" \
  "ds_read_b128 v[56:59], v147 offset:1024\n\t" \
  "ds_read_b128 v[60:63], v147 offset:2048\n\t" \
  "ds_read_b128 v[64:67], v147 offset:3072\n\t" \
  "ds_read_b128 v[68:71], v147 offset:4096\n\t" \
  "ds_read_b128 v[72:75], v147 offset:5120\n\t" \
  "ds_read_b128 v[76:79], v147 offset:6144\n\t" \
  "ds_read_b128 v[80:83], v147 offset:7168\n\t" \
  "v_mov_b64 v[116:117], -1\n\t" \
  "v_mov_b64 v[118:119], -1\n\t" \
  "s_add_u32 s70, s70, s52\n\t" \
  ".LH2w2%=:\n\t" \
  "s_memrealtime s[66:67]\n\t" \
  "s_waitcnt lgkmcnt(0)\n\t" \
  "s_sub_u32 s71, s66, s98\n\t" \
  "s_cmp_lt_i32 s71, 0\n\t" \
  "s_cbranch_scc1 .LH2w2%=\n\t" \
  "buffer_load_dwordx4 v[0:3], v139, s[40:43], s70 offen sc1\n\t" \
  "buffer_load_dwordx4 v[4:7], v139, s[40:43], s70 offen offset:1024 sc1\n\t" \
  "s_barrier\n\t" \
  "buffer_store_dwordx4 v[116:119], v139, s[40:43], s68 offen sc1\n\t" \
  "buffer_store_dword v130, v150, s[40:43], s57 offen sc1\n\t" \
  "buffer_store_dwordx4 v[8:11], v151, s[40:43], s58 offen sc1\n\t" \
  "s_mov_b32 s75, s90\n\t" \
  "s_add_u32 s90, s90, 128\n\t" \
  "s_add_u32 s68, s61, 4\n\t" \
  "s_lshr_b32 s68, s68, 1\n\t" \
  "s_cmp_ge_u32 s61, 62\n\t" \
  "s_cselect_b32 s68, 0, s68\n\t" \
  "s_waitcnt vmcnt(5)\n\t" \
  "v_readfirstlane_b32 s69, v128\n\t" \
  "s_cmp_lt_u32 s69, s68\n\t" \
  "s_cbranch_scc1 .LH2kvwait%=\n\t" \
  ".LH2kvok%=:\n\t" \
  "s_add_u32 m0, s77, 6144\n\t" \
  "s_nop 0\n\t" \
  "buffer_load_dwordx4 v139, s[40:43], s53 offen sc1 lds\n\t" \
  "s_add_u32 m0, s77, 7168\n\t" \
  "s_nop 0\n\t" \
  "buffer_load_dwordx4 v139, s[40:43], s54 offen sc1 lds\n\t" \
  "s_add_u32 m0, s77, 8192\n\t" \
  "s_nop 0\n\t" \
  "buffer_load_dwordx4 v139, s[40:43], s55 offen sc1 lds\n\t" \
  "s_xor_b32 s77, s77, 0xc000\n\t" \
  "s_xor_b32 s56, s56, 128\n\t" \
  "s_and_b32 s69, s61, 3\n\t" \
  "s_cmp_eq_u32 s69, 1\n\t" \
  "s_cselect_b32 s71, 0x2000, 0\n\t" \
  "s_add_u32 s53, s53, 0x80000\n\t" \
  "s_xor_b32 s53, s53, s71\n\t" \
  "s_add_u32 s54, s54, 0x80000\n\t" \
  "s_xor_b32 s54, s54, s71\n\t" \
  "s_add_u32 s55, s55, 0x80000\n\t" \
  "s_xor_b32 s55, s55, s71\n\t" \
  "s_cmp_eq_u32 s69, 3\n\t" \
  "s_cselect_b32 s71, 0x2000, 0\n\t" \
  "s_add_u32 s57, s57, 0x80000\n\t" \
  "s_xor_b32 s57, s57, s71\n\t" \
  "s_add_u32 s58, s58, 0x80000\n\t" \
  "s_xor_b32 s58, s58, s71\n\t" \
  "v_swap_b32 v146, v147\n\t" \
  "s_mov_b32 s71, s48\n\t" \
  "s_mov_b32 s48, s49\n\t" \
  "s_mov_b32 s49, s71\n\t" \
  "s_add_u32 s61, s61, 1\n\t" \
  "s_branch .LH2top%=\n\t" \
  ".LH2faila%=:\n\t" \
  "v_max3_u32 v129, v116, v117, v118\n\t" \
  "v_max_u32 v129, v129, v119\n\t" \
  "v_cmp_eq_u32 vcc, -1, v129\n\t" \
  "s_cbranch_vccz .LH2oka%=\n\t" \
  "s_add_u32 s83, s83, 1\n\t" \
  "s_add_u32 s71, s61, 1\n\t" \
  "v_mov_b32 v129, s71\n\t" \
  "ds_write_b32 v139, v129 offset:45056\n\t" \
  "s_cmp_lg_u32 s62, 0\n\t" \
  "s_cbranch_scc1 .LH2oka%=\n\t" \
  "s_add_u32 s63, s63, 1\n\t" \
  "s_and_b32 s71, s63, 255\n\t" \
  "s_cmp_lg_u32 s71, 0\n\t" \
  "s_cbranch_scc1 .LH2sga%=\n\t" \
  "buffer_load_dword v129, off, s[44:47], 0 sc1\n\t" \
  "s_waitcnt vmcnt(0)\n\t" \
  "v_readfirstlane_b32 s62, v129\n\t" \
  "s_cmp_lt_u32 s63, 0x100000\n\t" \
  "s_cbranch_scc1 .LH2sga%=\n\t" \
  "s_mov_b32 s62, 1\n\t" \
  "v_mov_b32 v129, 1\n\t" \
  "buffer_store_dword v129, off, s[44:47], 0 sc1\n\t" \
  ".LH2sga%=:\n\t" \
  "s_sleep 3\n\t" \
  "buffer_load_dwordx4 v[116:119], v139, s[40:43], s68 offen sc1\n\t" \
  "s_memrealtime s[66:67]\n\t" \
  "s_waitcnt lgkmcnt(0)\n\t" \
  "s_add_u32 s69, s66, s60\n\t" \
  "s_add_u32 s69, s69, s73\n\t" \
  "s_add_u32 s69, s69, s93\n\t" \
  "s_add_u32 s69, s69, s74\n\t" \
  "s_max_u32 s69, s69, 1\n\t" \
  "v_mov_b32 v132, s69\n\t" \
  "buffer_store_dword v132, v153, s[40:43], s90 offen sc1\n\t" \
  "s_waitcnt vmcnt(0)\n\t" \
  "s_branch .LH2redoa%=\n\t" \
  ".LH2failb%=:\n\t" \
  "s_add_u32 s84, s84, 1\n\t" \
  "v_mov_b32 v129, s61\n\t" \
  "ds_write_b32 v139, v129 offset:45056\n\t" \
  "s_cmp_lg_u32 s62, 0\n\t" \
  "s_cbranch_scc1 .LH2okb%=\n\t" \
  "s_add_u32 s63, s63, 1\n\t" \
  "s_and_b32 s71, s63, 255\n\t" \
  "s_cmp_lg_u32 s71, 0\n\t" \
  "s_cbranch_scc1 .LH2sgb%=\n\t" \
  "buffer_load_dword v129, off, s[44:47], 0 sc1\n\t" \
  "s_waitcnt vmcnt(0)\n\t" \
  "v_readfirstlane_b32 s62, v129\n\t" \
  "s_cmp_lt_u32 s63, 0x100000\n\t" \
  "s_cbranch_scc1 .LH2sgb%=\n\t" \
  "s_mov_b32 s62, 1\n\t" \
  "v_mov_b32 v129, 1\n\t" \
  "buffer_store_dword v129, off, s[44:47], 0 sc1\n\t" \
  ".LH2sgb%=:\n\t" \
  "s_sleep 3\n\t" \
  "buffer_load_dwordx4 v[0:3], v139, s[40:43], s70 offen sc1\n\t" \
  "buffer_load_dwordx4 v[4:7], v139, s[40:43], s70 offen offset:1024 sc1\n\t" \
  "s_memrealtime s[66:67]\n\t" \
  "s_waitcnt lgkmcnt(0)\n\t" \
  "s_add_u32 s69, s66, s73\n\t" \
  "s_add_u32 s69, s69, s93\n\t" \
  "s_add_u32 s69, s69, s74\n\t" \
  "s_max_u32 s69, s69, 1\n\t" \
  "v_mov_b32 v132, s69\n\t" \
  "buffer_store_dword v132, v153, s[40:43], s75 offen sc1\n\t" \
  "s_nop 8\n\t" \
  "v_mov_b64 v[32:33], 0\n\t" \
  "v_mov_b64 v[34:35], 0\n\t" \
  "v_mov_b64 v[36:37], 0\n\t" \
  "v_mov_b64 v[38:39], 0\n\t" \
  "v_mov_b64 v[40:41], 0\n\t" \
  "v_mov_b64 v[42:43], 0\n\t" \
  "v_mov_b64 v[44:45], 0\n\t" \
  "v_mov_b64 v[46:47], 0\n\t" \
  "s_waitcnt vmcnt(0)\n\t" \
  "s_branch .LH2redo%=\n\t" \
  ".LH2bigm%=:\n\t" \
  "v_mul_f32 v37, s81, v124\n\t" \
  "v_max_f32 v34, v34, v37\n\t" \
  "s_nop 1\n\t" \
  "v_max_f32_dpp v34, v34, v34 row_ror:8 row_mask:0xf bank_mask:0xf\n\t" \
  "v_mov_b32 v33, v34\n\t" \
  "s_nop 1\n\t" \
  "v_permlane32_swap_b32 v34, v33\n\t" \
  "v_max_f32 v34, v34, v33\n\t" \
  "v_mov_b32 v33, v34\n\t" \
  "s_nop 1\n\t" \
  "v_permlane16_swap_b32 v34, v33\n\t" \
  "v_max_f32 v34, v34, v33\n\t" \
  "v_fma_f32 v120, v116, s81, -v34\n\t" \
  "v_fma_f32 v121, v117, s81, -v34\n\t" \
  "v_fma_f32 v122, v118, s81, -v34\n\t" \
  "v_fma_f32 v123, v119, s81, -v34\n\t" \
  "v_fma_f32 v37, v124, s81, -v34\n\t" \
  "v_exp_f32 v120, v120\n\t" \
  "v_exp_f32 v121, v121\n\t" \
  "v_exp_f32 v122, v122\n\t" \
  "v_exp_f32 v123, v123\n\t" \
  "v_exp_f32 v37, v37\n\t" \
  "v_cvt_pk_bf16_f32 v108, v120, v121\n\t" \
  "v_cvt_pk_bf16_f32 v109, v122, v123\n\t" \
  "v_mov_b32 v110, v108\n\t" \
  "v_mov_b32 v111, v109\n\t" \
  "v_mov_b32_dpp v108, v108 row_ror:8 row_mask:0xf bank_mask:0xc\n\t" \
  "v_mov_b32_dpp v109, v109 row_ror:8 row_mask:0xf bank_mask:0xc\n\t" \
  "v_mov_b32_dpp v110, v110 row_ror:8 row_mask:0xf bank_mask:0x3\n\t" \
  "v_mov_b32_dpp v111, v111 row_ror:8 row_mask:0xf bank_mask:0x3\n\t" \
  "s_nop 7\n\t" \
  "v_mfma_f32_16x16x32_bf16 v[52:55], v[0:3], v[108:111], 0\n\t" \
  "v_mfma_f32_16x16x32_bf16 v[56:59], v[4:7], v[108:111], 0\n\t" \
  "v_mfma_f32_16x16x32_bf16 v[60:63], v[8:11], v[108:111], 0\n\t" \
  "v_mfma_f32_16x16x32_bf16 v[64:67], v[12:15], v[108:111], 0\n\t" \
  "s_branch .LH2bigr%=\n\t" \
  ".LH2kvwait%=:\n\t" \
  "buffer_load_dword v128, off, s[44:47], s56 sc1\n\t" \
  "s_waitcnt vmcnt(0)\n\t" \
  "v_readfirstlane_b32 s69, v128\n\t" \
  "s_cmp_ge_u32 s69, s68\n\t" \
  "s_cbranch_scc1 .LH2kvok%=\n\t" \
  "s_sleep 4\n\t" \
  "s_cmp_lg_u32 s62, 0\n\t" \
  "s_cbranch_scc1 .LH2kvok%=\n\t" \
  "s_add_u32 s63, s63, 1\n\t" \
  "s_and_b32 s71, s63, 255\n\t" \
  "s_cmp_lg_u32 s71, 0\n\t" \
  "s_cbranch_scc1 .LH2sgk%=\n\t" \
  "buffer_load_dword v129, off, s[44:47], 0 sc1\n\t" \
  "s_waitcnt vmcnt(0)\n\t" \
  "v_readfirstlane_b32 s62, v129\n\t" \
  "s_cmp_lt_u32 s63, 0x100000\n\t" \
  "s_cbranch_scc1 .LH2sgk%=\n\t" \
  "s_mov_b32 s62, 1\n\t" \
  "v_mov_b32 v129, 1\n\t" \
  "buffer_store_dword v129, off, s[44:47], 0 sc1\n\t" \
  ".LH2sgk%=:\n\t" \
  "s_branch .LH2kvwait%=\n\t" \
  ".LH2tmwait%=:\n\t" \
  "buffer_load_dword v132, off, s[40:43], s75 sc1\n\t" \
  "s_waitcnt vmcnt(0)\n\t" \
  "v_readfirstlane_b32 s70, v132\n\t" \
  "s_cmp_lg_u32 s70, 0\n\t" \
  "s_cbranch_scc1 .LH2tmok%=\n\t" \
  "s_mov_b32 s70, s92\n\t" \
  "s_cmp_lg_u32 s62, 0\n\t" \
  "s_cbranch_scc1 .LH2tmok%=\n\t" \
  "s_add_u32 s63, s63, 1\n\t" \
  "s_and_b32 s71, s63, 255\n\t" \
  "s_cmp_lg_u32 s71, 0\n\t" \
  "s_cbranch_scc1 .LH2sgt%=\n\t" \
  "buffer_load_dword v129, off, s[44:47], 0 sc1\n\t" \
  "s_waitcnt vmcnt(0)\n\t" \
  "v_readfirstlane_b32 s62, v129\n\t" \
  "s_cmp_lt_u32 s63, 0x100000\n\t" \
  "s_cbranch_scc1 .LH2sgt%=\n\t" \
  "s_mov_b32 s62, 1\n\t" \
  "v_mov_b32 v129, 1\n\t" \
  "buffer_store_dword v129, off, s[44:47], 0 sc1\n\t" \
  ".LH2sgt%=:\n\t" \
  "s_branch .LH2tmwait%=\n\t" \
  ".LH2last%=:\n\t" \
  "ds_read_b64_tr_b16 v[8:9], v141 offset:3072\n\t" \
  "ds_read_b64_tr_b16 v[10:11], v141 offset:3328\n\t" \
  "s_waitcnt lgkmcnt(0)\n\t" \
  "buffer_store_dword v130, v150, s[40:43], s57 offen sc1\n\t" \
  "buffer_store_dwordx4 v[8:11], v151, s[40:43], s58 offen sc1\n\t" \
  "s_waitcnt vmcnt(0)\n\t" \
  "s_barrier\n\t" \
  "v_mov_b32 v129, 64\n\t" \
  "buffer_store_dword v129, v152, s[44:47], 0 offen sc1\n\t" \
  "s_branch .LHend%=\n\t" \
  ".LH3start%=:\n\t" \
  "global_load_dword v0, v[128:129], off\n\t" \
  "global_load_dword v1, v[128:129], off offset:8\n\t" \
  "global_load_dword v2, v[128:129], off offset:16\n\t" \
  "global_load_dword v3, v[128:129], off offset:24\n\t" \
  "global_load_dword v4, v[128:129], off offset:128\n\t" \
  "global_load_dword v5, v[128:129], off offset:136\n\t" \
  "global_load_dword v6, v[128:129], off offset:144\n\t" \
  "global_load_dword v7, v[128:129], off offset:152\n\t" \
  "ds_read_b128 v[52:55], v146\n\t" \
  "ds_read_b128 v[56:59], v146 offset:1024\n\t" \
  "ds_read_b128 v[60:63], v146 offset:2048\n\t" \
  "ds_read_b128 v[64:67], v146 offset:3072\n\t" \
  "ds_read_b128 v[68:71], v146 offset:4096\n\t" \
  "ds_read_b128 v[72:75], v146 offset:5120\n\t" \
  "ds_read_b128 v[76:79], v146 offset:6144\n\t" \
  "ds_read_b128 v[80:83], v146 offset:7168\n\t" \
  "v_mov_b64 v[32:33], 0\n\t" \
  "v_mov_b64 v[34:35], 0\n\t" \
  "v_mov_b64 v[36:37], 0\n\t" \
  "v_mov_b64 v[38:39], 0\n\t" \
  "v_mov_b64 v[40:41], 0\n\t" \
  "v_mov_b64 v[42:43], 0\n\t" \
  "v_mov_b64 v[44:45], 0\n\t" \
  "v_mov_b64 v[46:47], 0\n\t" \
  "v_mov_b64 v[48:49], 0\n\t" \
  "v_mov_b64 v[50:51], 0\n\t" \
  "v_mov_b32 v131, 0\n\t" \
  "v_mov_b32 v137, 0\n\t" \
  "v_mov_b32 v154, 0x3f803f80\n\t" \
  "v_mov_b32 v155, 0x3f803f80\n\t" \
  "v_mov_b32 v156, 0x3f803f80\n\t" \
  "v_mov_b32 v157, 0x3f803f80\n\t" \
  "s_waitcnt vmcnt(0)\n\t" \
  "v_mov_b32 v128, 0\n\t" \
  "s_memrealtime s[66:67]\n\t" \
  "s_waitcnt lgkmcnt(0)\n\t" \
  "s_mov_b32 s91, s66\n\t" \
  "s_add_u32 s66, s66, s88\n\t" \
  "s_or_b32 s66, s66, 1\n\t" \
  "v_mov_b32 v132, s66\n\t" \
  "s_mov_b32 s88, -8\n\t" \
  "s_mov_b32 s77, 16384\n\t" \
  "s_mov_b32 s75, s90\n\t" \
  "s_add_u32 s90, s90, 128\n\t" \
  "s_mov_b32 s74, s89\n\t" \
  "s_nop 0\n\t" \
  "s_mov_b32 s73, s59\n\t" \
  "s_add_u32 s76, s76, s59\n\t" \
  "v_min_u32 v0, -2, v0\n\t" \
  "v_min_u32 v1, -2, v1\n\t" \
  "v_min_u32 v2, -2, v2\n\t" \
  "v_min_u32 v3, -2, v3\n\t" \
  "v_min_u32 v4, -2, v4\n\t" \
  "v_min_u32 v5, -2, v5\n\t" \
  "v_min_u32 v6, -2, v6\n\t" \
  "v_min_u32 v7, -2, v7\n\t" \
  "s_nop 1\n\t" \
  ".LH3top%=:\n\t" \
  "s_waitcnt vmcnt(6)\n\t" \
  ".LH3redo%=:\n\t" \
  "ds_write_b128 v139, v[0:3] offset:6144\n\t" \
  "v_smfmac_f32_16x16x64_bf16 v[32:35], v[0:3], a[48:55], v138\n\t" \
  "ds_write_b128 v139, v[4:7] offset:7168\n\t" \
  "v_smfmac_f32_16x16x64_bf16 v[36:39], v[0:3], a[112:119], v138\n\t" \
  "v_max3_u32 v129, v0, v1, v2\n\t" \
  "v_max3_u32 v129, v129, v3, v4\n\t" \
  "v_smfmac_f32_16x16x64_bf16 v[40:43], v[0:3], a[176:183], v138\n\t" \
  "v_max3_u32 v129, v129, v5, v6\n\t" \
  "v_max_u32 v129, v129, v7\n\t" \
  "v_smfmac_f32_16x16x64_bf16 v[44:47], v[0:3], a[240:247], v138\n\t" \
  "v_cmp_eq_u32 vcc, -1, v129\n\t" \
  "s_cbranch_vccnz .LH3failb%=\n\t" \
  ".LH3okb%=:\n\t" \
  "s_waitcnt lgkmcnt(0)\n\t" \
  "s_barrier\n\t" \
  "ds_read_b128 v[8:11], v139\n\t" \
  "v_smfmac_f32_16x16x64_bf16 v[32:35], v[4:7], a[56:63], v138\n\t" \
  "ds_read_b128 v[12:15], v139 offset:1024\n\t" \
  "v_smfmac_f32_16x16x64_bf16 v[36:39], v[4:7], a[120:127], v138\n\t" \
  "ds_read_b128 v[16:19], v139 offset:2048\n\t" \
  "v_smfmac_f32_16x16x64_bf16 v[40:43], v[4:7], a[184:191], v138\n\t" \
  "ds_read_b128 v[20:23], v139 offset:3072\n\t" \
  "v_smfmac_f32_16x16x64_bf16 v[44:47], v[4:7], a[248:255], v138\n\t" \
  "ds_read_b128 v[24:27], v139 offset:4096\n\t" \
  "v_smfmac_f32_16x16x64_bf16 v[48:51], v[0:3], v[208:215], v138\n\t" \
  "ds_read_b128 v[28:31], v139 offset:5120\n\t" \
  "v_smfmac_f32_16x16x64_bf16 v[48:51], v[4:7], v[216:223], v138\n\t" \
  "s_waitcnt lgkmcnt(5)\n\t" \
  "v_smfmac_f32_16x16x64_bf16 v[32:35], v[8:11], a[0:7], v138\n\t" \
  "v_smfmac_f32_16x16x64_bf16 v[36:39], v[8:11], a[64:71], v138\n\t" \
  "s_waitcnt lgkmcnt(4)\n\t" \
  "v_smfmac_f32_16x16x64_bf16 v[32:35], v[12:15], a[8:15], v138\n\t" \
  "v_smfmac_f32_16x16x64_bf16 v[36:39], v[12:15], a[72:79], v138\n\t" \
  "s_waitcnt lgkmcnt(3)\n\t" \
  "v_smfmac_f32_16x16x64_bf16 v[32:35], v[16:19], a[16:23], v138\n\t" \
  "v_smfmac_f32_16x16x64_bf16 v[36:39], v[16:19], a[80:87], v138\n\t" \
  "s_waitcnt lgkmcnt(2)\n\t" \
  "v_smfmac_f32_16x16x64_bf16 v[32:35], v[20:23], a[24:31], v138\n\t" \
  "v_smfmac_f32_16x16x64_bf16 v[36:39], v[20:23], a[88:95], v138\n\t" \
  "s_waitcnt lgkmcnt(1)\n\t" \
  "v_smfmac_f32_16x16x64_bf16 v[32:35], v[24:27], a[32:39], v138\n\t" \
  "ds_read_b128 v[0:3], v146 offset:8192\n\t" \
  "v_smfmac_f32_16x16x64_bf16 v[36:39], v[24:27], a[96:103], v138\n\t" \
  "s_waitcnt lgkmcnt(1)\n\t" \
  "v_smfmac_f32_16x16x64_bf16 v[32:35], v[28:31], a[40:47], v138\n\t" \
  "ds_read_b128 v[4:7], v146 offset:9216\n\t" \
  "v_smfmac_f32_16x16x64_bf16 v[36:39], v[28:31], a[104:111], v138\n\t" \
  "v_smfmac_f32_16x16x64_bf16 v[48:51], v[8:11], v[160:167], v138\n\t" \
  "v_smfmac_f32_16x16x64_bf16 v[48:51], v[12:15], v[168:175], v138\n\t" \
  "s_nop 0\n\t" \
  "v_permlane32_swap_b32 v32, v36\n\t" \
  "v_smfmac_f32_16x16x64_bf16 v[48:51], v[16:19], v[176:183], v138\n\t" \
  "v_permlane32_swap_b32 v33, v37\n\t" \
  "v_smfmac_f32_16x16x64_bf16 v[48:51], v[20:23], v[184:191], v138\n\t" \
  "v_permlane32_swap_b32 v34, v38\n\t" \
  "v_smfmac_f32_16x16x64_bf16 v[48:51], v[24:27], v[192:199], v138\n\t" \
  "v_permlane32_swap_b32 v35, v39\n\t" \
  "v_smfmac_f32_16x16x64_bf16 v[48:51], v[28:31], v[200:207], v138\n\t" \
  "v_add_f32 v32, v32, v36\n\t" \
  "v_add_f32 v33, v33, v37\n\t" \
  "v_smfmac_f32_16x16x64_bf16 v[40:43], v[8:11], a[128:135], v138\n\t" \
  "v_add_f32 v34, v34, v38\n\t" \
  "v_add_f32 v35, v35, v39\n\t" \
  "v_smfmac_f32_16x16x64_bf16 v[44:47], v[8:11], a[192:199], v138\n\t" \
  "v_cvt_pk_bf16_f32 v32, v32, v33\n\t" \
  "v_cvt_pk_bf16_f32 v33, v34, v35\n\t" \
  "v_smfmac_f32_16x16x64_bf16 v[40:43], v[12:15], a[136:143], v138\n\t" \
  "ds_write_b64 v140, v[32:33]\n\t" \
  "v_smfmac_f32_16x16x64_bf16 v[44:47], v[12:15], a[200:207], v138\n\t" \
  "v_permlane32_swap_b32 v48, v49\n\t" \
  "v_smfmac_f32_16x16x64_bf16 v[40:43], v[16:19], a[144:151], v138\n\t" \
  "v_permlane32_swap_b32 v50, v51\n\t" \
  "v_smfmac_f32_16x16x64_bf16 v[44:47], v[16:19], a[208:215], v138\n\t" \
  "v_add_f32 v48, v48, v49\n\t" \
  "v_add_f32 v50, v50, v51\n\t" \
  "v_smfmac_f32_16x16x64_bf16 v[40:43], v[20:23], a[152:159], v138\n\t" \
  "v_cvt_pk_bf16_f32 v130, v48, v50\n\t" \
  "v_smfmac_f32_16x16x64_bf16 v[44:47], v[20:23], a[216:223], v138\n\t" \
  "s_waitcnt lgkmcnt(0)\n\t" \
  "s_barrier\n\t" \
  "ds_read_b64_tr_b16 v[84:85], v141\n\t" \
  "ds_read_b64_tr_b16 v[86:87], v141 offset:256\n\t" \
  "v_smfmac_f32_16x16x64_bf16 v[40:43], v[24:27], a[160:167], v138\n\t" \
  "ds_read_b64_tr_b16 v[88:89], v141 offset:512\n\t" \
  "ds_read_b64_tr_b16 v[90:91], v141 offset:768\n\t" \
  "v_smfmac_f32_16x16x64_bf16 v[44:47], v[24:27], a[224:231], v138\n\t" \
  "ds_read_b64_tr_b16 v[92:93], v141 offset:1024\n\t" \
  "ds_read_b64_tr_b16 v[94:95], v141 offset:1280\n\t" \
  "v_smfmac_f32_16x16x64_bf16 v[40:43], v[28:31], a[168:175], v138\n\t" \
  "ds_read_b64_tr_b16 v[96:97], v141 offset:1536\n\t" \
  "ds_read_b64_tr_b16 v[98:99], v141 offset:1792\n\t" \
  "v_smfmac_f32_16x16x64_bf16 v[44:47], v[28:31], a[232:239], v138\n\t" \
  "ds_write_b64 v142, v[130:131]\n\t" \
  "s_waitcnt lgkmcnt(7)\n\t" \
  "v_mfma_f32_16x16x32_bf16 v[116:119], v[52:55], v[84:87], 0\n\t" \
  "ds_read_b128 v[8:11], v146 offset:10240\n\t" \
  "v_mfma_f32_16x16x32_bf16 v[120:123], v[68:71], v[84:87], 0\n\t" \
  "v_permlane32_swap_b32 v40, v44\n\t" \
  "v_permlane32_swap_b32 v41, v45\n\t" \
  "s_waitcnt lgkmcnt(6)\n\t" \
  "v_mfma_f32_16x16x32_bf16 v[116:119], v[56:59], v[88:91], v[116:119]\n\t" \
  "v_permlane32_swap_b32 v42, v46\n\t" \
  "v_permlane32_swap_b32 v43, v47\n\t" \
  "v_mfma_f32_16x16x32_bf16 v[120:123], v[72:75], v[88:91], v[120:123]\n\t" \
  "v_add_f32 v40, v40, v44\n\t" \
  "v_add_f32 v41, v41, v45\n\t" \
  "s_waitcnt lgkmcnt(4)\n\t" \
  "v_mfma_f32_16x16x32_bf16 v[116:119], v[60:63], v[92:95], v[116:119]\n\t" \
  "v_add_f32 v42, v42, v46\n\t" \
  "v_add_f32 v43, v43, v47\n\t" \
  "v_mfma_f32_16x16x32_bf16 v[120:123], v[76:79], v[92:95], v[120:123]\n\t" \
  "v_cvt_pk_bf16_f32 v40, v40, v41\n\t" \
  "v_cvt_pk_bf16_f32 v41, v42, v43\n\t" \
  "s_waitcnt lgkmcnt(2)\n\t" \
  "v_mfma_f32_16x16x32_bf16 v[116:119], v[64:67], v[96:99], v[116:119]\n\t" \
  "ds_write_b64 v140, v[40:41] offset:2048\n\t" \
  "v_mfma_f32_16x16x32_bf16 v[120:123], v[80:83], v[96:99], v[120:123]\n\t" \
  "s_nop 7\n\t" \
  "v_mov_b32_dpp v116, v120 quad_perm:[0,1,2,3] row_mask:0xf bank_mask:0xc\n\t" \
  "v_mov_b32_dpp v117, v121 quad_perm:[0,1,2,3] row_mask:0xf bank_mask:0xc\n\t" \
  "v_mov_b32_dpp v118, v122 quad_perm:[0,1,2,3] row_mask:0xf bank_mask:0xc\n\t" \
  "v_mov_b32_dpp v119, v123 quad_perm:[0,1,2,3] row_mask:0xf bank_mask:0xc\n\t" \
  "v_max3_f32 v34, v116, v117, v118\n\t" \
  "v_max_f32 v34, v34, v119\n\t" \
  "s_cmp_eq_u32 s61, 63\n\t" \
  "s_waitcnt lgkmcnt(0)\n\t" \
  "s_barrier\n\t" \
  "s_cbranch_scc1 .LH3last%=\n\t" \
  "v_max_f32_dpp v34, v34, v34 row_ror:8 row_mask:0xf bank_mask:0xf\n\t" \
  "v_mov_b32 v33, v34\n\t" \
  "ds_read_b64_tr_b16 v[100:101], v141 offset:2048\n\t" \
  "ds_read_b64_tr_b16 v[102:103], v141 offset:2304\n\t" \
  "ds_read_b64_tr_b16 v[104:105], v141 offset:2560\n\t" \
  "ds_read_b64_tr_b16 v[106:107], v141 offset:2816\n\t" \
  "v_permlane32_swap_b32 v34, v33\n\t" \
  "v_max_f32 v34, v34, v33\n\t" \
  "v_mov_b32 v33, v34\n\t" \
  "ds_read_b64_tr_b16 v[108:109], v141 offset:3072\n\t" \
  "ds_read_b64_tr_b16 v[110:111], v141 offset:3328\n\t" \
  "ds_read_b64_tr_b16 v[112:113], v141 offset:3584\n\t" \
  "ds_read_b64_tr_b16 v[114:115], v141 offset:3840\n\t" \
  "ds_read_b128 v[12:15], v146 offset:11264\n\t" \
  "v_permlane16_swap_b32 v34, v33\n\t" \
  "v_max_f32 v34, v34, v33\n\t" \
  "v_mul_f32 v34, s81, v34\n\t" \
  "s_waitcnt lgkmcnt(7)\n\t" \
  "v_mfma_f32_16x16x32_bf16 v[124:127], v[100:103], v[84:87], 0\n\t" \
  "v_fma_f32 v120, v116, s81, -v34\n\t" \
  "v_fma_f32 v121, v117, s81, -v34\n\t" \
  "s_waitcnt lgkmcnt(5)\n\t" \
  "v_mfma_f32_16x16x32_bf16 v[124:127], v[104:107], v[88:91], v[124:127]\n\t" \
  "v_fma_f32 v122, v118, s81, -v34\n\t" \
  "v_fma_f32 v123, v119, s81, -v34\n\t" \
  "s_waitcnt lgkmcnt(3)\n\t" \
  "v_mfma_f32_16x16x32_bf16 v[124:127], v[108:111], v[92:95], v[124:127]\n\t" \
  "v_exp_f32 v120, v120\n\t" \
  "s_waitcnt lgkmcnt(1)\n\t" \
  "v_mfma_f32_16x16x32_bf16 v[124:127], v[112:115], v[96:99], v[124:127]\n\t" \
  "v_exp_f32 v121, v121\n\t" \
  "v_exp_f32 v122, v122\n\t" \
  "v_exp_f32 v123, v123\n\t" \
  "ds_read_b128 v[40:43], v143\n\t" \
  "v_cvt_pk_bf16_f32 v108, v120, v121\n\t" \
  "v_cvt_pk_bf16_f32 v109, v122, v123\n\t" \
  "v_mov_b32 v110, v108\n\t" \
  "v_mov_b32 v111, v109\n\t" \
  "v_mov_b32_dpp v108, v108 row_ror:8 row_mask:0xf bank_mask:0xc\n\t" \
  "v_mov_b32_dpp v109, v109 row_ror:8 row_mask:0xf bank_mask:0xc\n\t" \
  "v_mov_b32_dpp v110, v110 row_ror:8 row_mask:0xf bank_mask:0x3\n\t" \
  "v_mov_b32_dpp v111, v111 row_ror:8 row_mask:0xf bank_mask:0x3\n\t" \
  "v_mov_b32_dpp v124, v125 quad_perm:[0,1,2,3] row_mask:0xc bank_mask:0xf\n\t" \
  "v_mov_b32_dpp v126, v127 quad_perm:[0,1,2,3] row_mask:0xc bank_mask:0xf\n\t" \
  "v_mfma_f32_16x16x32_bf16 v[52:55], v[0:3], v[108:111], 0\n\t" \
  "ds_read_b128 v[44:47], v143 offset:16\n\t" \
  "v_mfma_f32_16x16x32_bf16 v[56:59], v[4:7], v[108:111], 0\n\t" \
  "s_waitcnt lgkmcnt(2)\n\t" \
  "v_mov_b32_dpp v124, v126 quad_perm:[0,1,2,3] row_mask:0xf bank_mask:0xc\n\t" \
  "v_fma_f32 v37, v124, s81, -v34\n\t" \
  "v_mfma_f32_16x16x32_bf16 v[60:63], v[8:11], v[108:111], 0\n\t" \
  "v_cmp_lt_f32 vcc, s79, v37\n\t" \
  "v_exp_f32 v37, v37\n\t" \
  "v_mfma_f32_16x16x32_bf16 v[64:67], v[12:15], v[108:111], 0\n\t" \
  "s_cbranch_vccnz .LH3bigm%=\n\t" \
  ".LH3bigr%=:\n\t" \
  "v_mov_b32 v38, v37\n\t" \
  "v_mfma_f32_16x16x32_bf16 v[76:79], v[108:111], v[154:157], 0\n\t" \
  "s_nop 0\n\t" \
  "v_mov_b32_dpp v37, v37 row_ror:8 row_mask:0xf bank_mask:0xc\n\t" \
  "v_mov_b32_dpp v38, v38 row_ror:8 row_mask:0xf bank_mask:0x3\n\t" \
  "v_cvt_pk_bf16_f32 v136, v37, v38\n\t" \
  "s_waitcnt lgkmcnt(0)\n\t" \
  "s_nop 0\n\t" \
  "v_mfma_f32_16x16x16_bf16 v[52:55], v[40:41], v[136:137], v[52:55]\n\t" \
  "s_mov_b64 exec, 1\n\t" \
  "buffer_load_dword v132, off, s[40:43], s75 sc1\n\t" \
  "s_mov_b64 exec, -1\n\t" \
  "v_mfma_f32_16x16x16_bf16 v[56:59], v[42:43], v[136:137], v[56:59]\n\t" \
  "v_mfma_f32_16x16x16_bf16 v[60:63], v[44:45], v[136:137], v[60:63]\n\t" \
  "v_mfma_f32_16x16x16_bf16 v[64:67], v[46:47], v[136:137], v[64:67]\n\t" \
  "v_cvt_pk_bf16_f32 v68, v52, v53\n\t" \
  "v_cvt_pk_bf16_f32 v69, v54, v55\n\t" \
  "v_mfma_f32_16x16x16_bf16 v[76:79], v[136:137], v[154:155], v[76:79]\n\t" \
  "v_cvt_pk_bf16_f32 v70, v56, v57\n\t" \
  "v_cvt_pk_bf16_f32 v71, v58, v59\n\t" \
  "v_cvt_pk_bf16_f32 v72, v60, v61\n\t" \
  "v_cvt_pk_bf16_f32 v73, v62, v63\n\t" \
  "v_mfma_f32_16x16x32_bf16 v[84:87], v[68:71], v[224:227], 0\n\t" \
  "v_cvt_pk_bf16_f32 v74, v64, v65\n\t" \
  "v_cvt_pk_bf16_f32 v75, v66, v67\n\t" \
  "v_mfma_f32_16x16x32_bf16 v[88:91], v[68:71], v[232:235], 0\n\t" \
  "s_add_u32 s68, s48, 65536\n\t" \
  "v_rcp_f32 v76, v76\n\t" \
  "v_mfma_f32_16x16x32_bf16 v[84:87], v[72:75], v[228:231], v[84:87]\n\t" \
  "v_rcp_f32 v77, v77\n\t" \
  "v_mfma_f32_16x16x32_bf16 v[88:91], v[72:75], v[236:239], v[88:91]\n\t" \
  "v_rcp_f32 v78, v78\n\t" \
  "v_mfma_f32_16x16x32_bf16 v[92:95], v[68:71], v[240:243], 0\n\t" \
  "v_rcp_f32 v79, v79\n\t" \
  "v_mfma_f32_16x16x32_bf16 v[96:99], v[68:71], v[248:251], 0\n\t" \
  "s_nop 0\n\t" \
  "v_mov_b32_dpp v84, v88 quad_perm:[0,1,2,3] row_mask:0xc bank_mask:0xf\n\t" \
  "v_mfma_f32_16x16x32_bf16 v[92:95], v[72:75], v[244:247], v[92:95]\n\t" \
  "v_mov_b32_dpp v85, v89 quad_perm:[0,1,2,3] row_mask:0xc bank_mask:0xf\n\t" \
  "v_mov_b32_dpp v86, v90 quad_perm:[0,1,2,3] row_mask:0xc bank_mask:0xf\n\t" \
  "v_mfma_f32_16x16x32_bf16 v[96:99], v[72:75], v[252:255], v[96:99]\n\t" \
  "v_mov_b32_dpp v87, v91 quad_perm:[0,1,2,3] row_mask:0xc bank_mask:0xf\n\t" \
  "v_mul_f32 v84, v84, v76\n\t" \
  "v_mul_f32 v85, v85, v77\n\t" \
  "v_mul_f32 v86, v86, v78\n\t" \
  "v_mul_f32 v87, v87, v79\n\t" \
  "v_cvt_pk_bf16_f32 v84, v84, v85\n\t" \
  "v_cvt_pk_bf16_f32 v85, v86, v87\n\t" \
  "buffer_store_dwordx2 v[84:85], v148, s[40:43], s48 offen sc1\n\t" \
  "v_mov_b32_dpp v92, v96 quad_perm:[0,1,2,3] row_mask:0xc bank_mask:0xf\n\t" \
  "v_mov_b32_dpp v93, v97 quad_perm:[0,1,2,3] row_mask:0xc bank_mask:0xf\n\t" \
  "v_mov_b32_dpp v94, v98 quad_perm:[0,1,2,3] row_mask:0xc bank_mask:0xf\n\t" \
  "v_mov_b32_dpp v95, v99 quad_perm:[0,1,2,3] row_mask:0xc bank_mask:0xf\n\t" \
  "v_mul_f32 v92, v92, v76\n\t" \
  "v_mul_f32 v93, v93, v77\n\t" \
  "v_mul_f32 v94, v94, v78\n\t" \
  "v_mul_f32 v95, v95, v79\n\t" \
  "v_cvt_pk_bf16_f32 v92, v92, v93\n\t" \
  "v_cvt_pk_bf16_f32 v93, v94, v95\n\t" \
  "buffer_store_dwordx2 v[92:93], v148, s[40:43], s68 offen sc1\n\t" \
  "s_memrealtime s[64:65]\n\t" \
  "ds_read_b64_tr_b16 v[8:9], v141 offset:3584\n\t" \
  "ds_read_b64_tr_b16 v[10:11], v141 offset:3840\n\t" \
  "ds_read_b128 v[120:123], v139 offset:15360\n\t" \
  "ds_read_b32 v129, v139 offset:45056\n\t" \
  "v_mov_b64 v[32:33], 0\n\t" \
  "v_mov_b64 v[34:35], 0\n\t" \
  "v_mov_b64 v[36:37], 0\n\t" \
  "v_mov_b64 v[38:39], 0\n\t" \
  "v_mov_b64 v[40:41], 0\n\t" \
  "v_mov_b64 v[42:43], 0\n\t" \
  "v_mov_b64 v[44:45], 0\n\t" \
  "v_mov_b64 v[46:47], 0\n\t" \
  "v_mov_b64 v[48:49], 0\n\t" \
  "v_mov_b64 v[50:51], 0\n\t" \
  "s_add_u32 s68, s48, s51\n\t" \
  "s_waitcnt vmcnt(2)\n\t" \
  "v_readfirstlane_b32 s70, v132\n\t" \
  "s_waitcnt lgkmcnt(0)\n\t" \
  "v_readfirstlane_b32 s72, v129\n\t" \
  "s_cmp_eq_u32 s61, 0\n\t" \
  "s_cselect_b32 s72, 0, s72\n\t" \
  "s_mov_b32 s92, s64\n\t" \
  "s_cmp_eq_u32 s70, 0\n\t" \
  "s_cbranch_scc1 .LH3tmwait%=\n\t" \
  ".LH3tmok%=:\n\t" \
  ".LH3w1%=:\n\t" \
  "s_memrealtime s[66:67]\n\t" \
  "s_waitcnt lgkmcnt(0)\n\t" \
  "s_sub_u32 s69, s66, s70\n\t" \
  "s_cmp_lt_i32 s69, 0\n\t" \
  "s_cbranch_scc1 .LH3w1%=\n\t" \
  "buffer_load_dwordx4 v[116:119], v139, s[40:43], s68 offen sc1\n\t" \
  "s_add_u32 s98, s70, s60\n\t" \
  "s_barrier\n\t" \
  "s_sub_u32 s69, s92, s91\n\t" \
  "s_mov_b32 s91, s98\n\t" \
  "s_sub_u32 s71, s61, s88\n\t" \
  "s_cmp_le_u32 s71, 3\n\t" \
  "s_cselect_b32 s71, 2, 0\n\t" \
  "s_min_u32 s69, s69, s76\n\t" \
  "s_cmp_lg_u32 s72, s61\n\t" \
  "s_cselect_b32 s73, s69, s73\n\t" \
  "s_cselect_b32 s76, s69, s76\n\t" \
  "s_cselect_b32 s88, s88, s61\n\t" \
  "s_cselect_b32 s71, 0, s71\n\t" \
  "s_add_u32 s93, s93, s71\n\t" \
  "buffer_load_dword v128, off, s[44:47], s56 sc1\n\t" \
  "s_add_u32 s69, s61, 1\n\t" \
  "s_and_b32 s69, s69, 3\n\t" \
  "s_lshl_b32 s69, s69, 16\n\t" \
  "s_add_u32 s70, s69, s50\n\t" \
  "s_waitcnt vmcnt(1)\n\t" \
  ".LH3redoa%=:\n\t" \
  "v_mfma_f32_16x16x32_bf16 v[124:127], v[116:119], v[120:123], 0\n\t" \
  "s_nop 7\n\t" \
  "v_permlane32_swap_b32 v124, v125\n\t" \
  "v_permlane32_swap_b32 v126, v127\n\t" \
  "v_add_f32 v124, v124, v125\n\t" \
  "v_add_f32 v126, v126, v127\n\t" \
  "s_nop 1\n\t" \
  "v_permlane16_swap_b32 v124, v126\n\t" \
  "v_add_f32 v124, v124, v126\n\t" \
  "ds_write_b32 v144, v124\n\t" \
  "v_cmp_u_f32 vcc, v124, v124\n\t" \
  "s_cbranch_vccnz .LH3faila%=\n\t" \
  ".LH3oka%=:\n\t" \
  "s_waitcnt lgkmcnt(0)\n\t" \
  "s_barrier\n\t" \
  "ds_read_b128 v[52:55], v147\n\t" \
  "ds_read_b128 v[56:59], v147 offset:1024\n\t" \
  "ds_read_b128 v[60:63], v147 offset:2048\n\t" \
  "ds_read_b128 v[64:67], v147 offset:3072\n\t" \
  "ds_read_b128 v[68:71], v147 offset:4096\n\t" \
  "ds_read_b128 v[72:75], v147 offset:5120\n\t" \
  "ds_read_b128 v[76:79], v147 offset:6144\n\t" \
  "ds_read_b128 v[80:83], v147 offset:7168\n\t" \
  "v_mov_b64 v[116:117], -1\n\t" \
  "v_mov_b64 v[118:119], -1\n\t" \
  "s_add_u32 s70, s70, s52\n\t" \
  ".LH3w2%=:\n\t" \
  "s_memrealtime s[66:67]\n\t" \
  "s_waitcnt lgkmcnt(0)\n\t" \
  "s_sub_u32 s71, s66, s98\n\t" \
  "s_cmp_lt_i32 s71, 0\n\t" \
  "s_cbranch_scc1 .LH3w2%=\n\t" \
  "buffer_load_dwordx4 v[0:3], v139, s[40:43], s70 offen sc1\n\t" \
  "buffer_load_dwordx4 v[4:7], v139, s[40:43], s70 offen offset:1024 sc1\n\t" \
  "s_barrier\n\t" \
  "buffer_store_dwordx4 v[116:119], v139, s[40:43], s68 offen sc1\n\t" \
  "buffer_store_dword v130, v150, s[40:43], s57 offen sc1\n\t" \
  "buffer_store_dwordx4 v[8:11], v151, s[40:43], s58 offen sc1\n\t" \
  "s_mov_b32 s75, s90\n\t" \
  "s_add_u32 s90, s90, 128\n\t" \
  "s_add_u32 s68, s61, 4\n\t" \
  "s_lshr_b32 s68, s68, 1\n\t" \
  "s_cmp_ge_u32 s61, 62\n\t" \
  "s_cselect_b32 s68, 0, s68\n\t" \
  "s_waitcnt vmcnt(5)\n\t" \
  "v_readfirstlane_b32 s69, v128\n\t" \
  "s_cmp_lt_u32 s69, s68\n\t" \
  "s_cbranch_scc1 .LH3kvwait%=\n\t" \
  ".LH3kvok%=:\n\t" \
  "s_add_u32 m0, s77, 9216\n\t" \
  "s_nop 0\n\t" \
  "buffer_load_dwordx4 v139, s[40:43], s53 offen sc1 lds\n\t" \
  "s_add_u32 m0, s77, 10240\n\t" \
  "s_nop 0\n\t" \
  "buffer_load_dwordx4 v139, s[40:43], s54 offen sc1 lds\n\t" \
  "s_add_u32 m0, s77, 11264\n\t" \
  "s_nop 0\n\t" \
  "buffer_load_dwordx4 v139, s[40:43], s55 offen sc1 lds\n\t" \
  "s_xor_b32 s77, s77, 0xc000\n\t" \
  "s_xor_b32 s56, s56, 128\n\t" \
  "s_and_b32 s69, s61, 3\n\t" \
  "s_cmp_eq_u32 s69, 1\n\t" \
  "s_cselect_b32 s71, 0x2000, 0\n\t" \
  "s_add_u32 s53, s53, 0x80000\n\t" \
  "s_xor_b32 s53, s53, s71\n\t" \
  "s_add_u32 s54, s54, 0x80000\n\t" \
  "s_xor_b32 s54, s54, s71\n\t" \
  "s_add_u32 s55, s55, 0x80000\n\t" \
  "s_xor_b32 s55, s55, s71\n\t" \
  "s_cmp_eq_u32 s69, 3\n\t" \
  "s_cselect_b32 s71, 0x2000, 0\n\t" \
  "s_add_u32 s57, s57, 0x80000\n\t" \
  "s_xor_b32 s57, s57, s71\n\t" \
  "s_add_u32 s58, s58, 0x80000\n\t" \
  "s_xor_b32 s58, s58, s71\n\t" \
  "v_swap_b32 v146, v147\n\t" \
  "s_mov_b32 s71, s48\n\t" \
  "s_mov_b32 s48, s49\n\t" \
  "s_mov_b32 s49, s71\n\t" \
  "s_add_u32 s61, s61, 1\n\t" \
  "s_branch .LH3top%=\n\t" \
  ".LH3faila%=:\n\t" \
  "v_max3_u32 v129, v116, v117, v118\n\t" \
  "v_max_u32 v129, v129, v119\n\t" \
  "v_cmp_eq_u32 vcc, -1, v129\n\t" \
  "s_cbranch_vccz .LH3oka%=\n\t" \
  "s_add_u32 s83, s83, 1\n\t" \
  "s_add_u32 s71, s61, 1\n\t" \
  "v_mov_b32 v129, s71\n\t" \
  "ds_write_b32 v139, v129 offset:45056\n\t" \
  "s_cmp_lg_u32 s62, 0\n\t" \
  "s_cbranch_scc1 .LH3oka%=\n\t" \
  "s_add_u32 s63, s63, 1\n\t" \
  "s_and_b32 s71, s63, 255\n\t" \
  "s_cmp_lg_u32 s71, 0\n\t" \
  "s_cbranch_scc1 .LH3sga%=\n\t" \
  "buffer_load_dword v129, off, s[44:47], 0 sc1\n\t" \
  "s_waitcnt vmcnt(0)\n\t" \
  "v_readfirstlane_b32 s62, v129\n\t" \
  "s_cmp_lt_u32 s63, 0x100000\n\t" \
  "s_cbranch_scc1 .LH3sga%=\n\t" \
  "s_mov_b32 s62, 1\n\t" \
  "v_mov_b32 v129, 1\n\t" \
  "buffer_store_dword v129, off, s[44:47], 0 sc1\n\t" \
  ".LH3sga%=:\n\t" \
  "s_sleep 3\n\t" \
  "buffer_load_dwordx4 v[116:119], v139, s[40:43], s68 offen sc1\n\t" \
  "s_memrealtime s[66:67]\n\t" \
  "s_waitcnt lgkmcnt(0)\n\t" \
  "s_add_u32 s69, s66, s60\n\t" \
  "s_add_u32 s69, s69, s73\n\t" \
  "s_add_u32 s69, s69, s93\n\t" \
  "s_add_u32 s69, s69, s74\n\t" \
  "s_max_u32 s69, s69, 1\n\t" \
  "v_mov_b32 v132, s69\n\t" \
  "buffer_store_dword v132, v153, s[40:43], s90 offen sc1\n\t" \
  "s_waitcnt vmcnt(0)\n\t" \
  "s_branch .LH3redoa%=\n\t" \
  ".LH3failb%=:\n\t" \
  "s_add_u32 s84, s84, 1\n\t" \
  "v_mov_b32 v129, s61\n\t" \
  "ds_write_b32 v139, v129 offset:45056\n\t" \
  "s_cmp_lg_u32 s62, 0\n\t" \
  "s_cbranch_scc1 .LH3okb%=\n\t" \
  "s_add_u32 s63, s63, 1\n\t" \
  "s_and_b32 s71, s63, 255\n\t" \
  "s_cmp_lg_u32 s71, 0\n\t" \
  "s_cbranch_scc1 .LH3sgb%=\n\t" \
  "buffer_load_dword v129, off, s[44:47], 0 sc1\n\t" \
  "s_waitcnt vmcnt(0)\n\t" \
  "v_readfirstlane_b32 s62, v129\n\t" \
  "s_cmp_lt_u32 s63, 0x100000\n\t" \
  "s_cbranch_scc1 .LH3sgb%=\n\t" \
  "s_mov_b32 s62, 1\n\t" \
  "v_mov_b32 v129, 1\n\t" \
  "buffer_store_dword v129, off, s[44:47], 0 sc1\n\t" \
  ".LH3sgb%=:\n\t" \
  "s_sleep 3\n\t" \
  "buffer_load_dwordx4 v[0:3], v139, s[40:43], s70 offen sc1\n\t" \
  "buffer_load_dwordx4 v[4:7], v139, s[40:43], s70 offen offset:1024 sc1\n\t" \
  "s_memrealtime s[66:67]\n\t" \
  "s_waitcnt lgkmcnt(0)\n\t" \
  "s_add_u32 s69, s66, s73\n\t" \
  "s_add_u32 s69, s69, s93\n\t" \
  "s_add_u32 s69, s69, s74\n\t" \
  "s_max_u32 s69, s69, 1\n\t" \
  "v_mov_b32 v132, s69\n\t" \
  "buffer_store_dword v132, v153, s[40:43], s75 offen sc1\n\t" \
  "s_nop 8\n\t" \
  "v_mov_b64 v[32:33], 0\n\t" \
  "v_mov_b64 v[34:35], 0\n\t" \
  "v_mov_b64 v[36:37], 0\n\t" \
  "v_mov_b64 v[38:39], 0\n\t" \
  "v_mov_b64 v[40:41], 0\n\t" \
  "v_mov_b64 v[42:43], 0\n\t" \
  "v_mov_b64 v[44:45], 0\n\t" \
  "v_mov_b64 v[46:47], 0\n\t" \
  "s_waitcnt vmcnt(0)\n\t" \
  "s_branch .LH3redo%=\n\t" \
  ".LH3bigm%=:\n\t" \
  "v_mul_f32 v37, s81, v124\n\t" \
  "v_max_f32 v34, v34, v37\n\t" \
  "s_nop 1\n\t" \
  "v_max_f32_dpp v34, v34, v34 row_ror:8 row_mask:0xf bank_mask:0xf\n\t" \
  "v_mov_b32 v33, v34\n\t" \
  "s_nop 1\n\t" \
  "v_permlane32_swap_b32 v34, v33\n\t" \
  "v_max_f32 v34, v34, v33\n\t" \
  "v_mov_b32 v33, v34\n\t" \
  "s_nop 1\n\t" \
  "v_permlane16_swap_b32 v34, v33\n\t" \
  "v_max_f32 v34, v34, v33\n\t" \
  "v_fma_f32 v120, v116, s81, -v34\n\t" \
  "v_fma_f32 v121, v117, s81, -v34\n\t" \
  "v_fma_f32 v122, v118, s81, -v34\n\t" \
  "v_fma_f32 v123, v119, s81, -v34\n\t" \
  "v_fma_f32 v37, v124, s81, -v34\n\t" \
  "v_exp_f32 v120, v120\n\t" \
  "v_exp_f32 v121, v121\n\t" \
  "v_exp_f32 v122, v122\n\t" \
  "v_exp_f32 v123, v123\n\t" \
  "v_exp_f32 v37, v37\n\t" \
  "v_cvt_pk_bf16_f32 v108, v120, v121\n\t" \
  "v_cvt_pk_bf16_f32 v109, v122, v123\n\t" \
  "v_mov_b32 v110, v108\n\t" \
  "v_mov_b32 v111, v109\n\t" \
  "v_mov_b32_dpp v108, v108 row_ror:8 row_mask:0xf bank_mask:0xc\n\t" \
  "v_mov_b32_dpp v109, v109 row_ror:8 row_mask:0xf bank_mask:0xc\n\t" \
  "v_mov_b32_dpp v110, v110 row_ror:8 row_mask:0xf bank_mask:0x3\n\t" \
  "v_mov_b32_dpp v111, v111 row_ror:8 row_mask:0xf bank_mask:0x3\n\t" \
  "s_nop 7\n\t" \
  "v_mfma_f32_16x16x32_bf16 v[52:55], v[0:3], v[108:111], 0\n\t" \
  "v_mfma_f32_16x16x32_bf16 v[56:59], v[4:7], v[108:111], 0\n\t" \
  "v_mfma_f32_16x16x32_bf16 v[60:63], v[8:11], v[108:111], 0\n\t" \
  "v_mfma_f32_16x16x32_bf16 v[64:67], v[12:15], v[108:111], 0\n\t" \
  "s_branch .LH3bigr%=\n\t" \
  ".LH3kvwait%=:\n\t" \
  "buffer_load_dword v128, off, s[44:47], s56 sc1\n\t" \
  "s_waitcnt vmcnt(0)\n\t" \
  "v_readfirstlane_b32 s69, v128\n\t" \
  "s_cmp_ge_u32 s69, s68\n\t" \
  "s_cbranch_scc1 .LH3kvok%=\n\t" \
  "s_sleep 4\n\t" \
  "s_cmp_lg_u32 s62, 0\n\t" \
  "s_cbranch_scc1 .LH3kvok%=\n\t" \
  "s_add_u32 s63, s63, 1\n\t" \
  "s_and_b32 s71, s63, 255\n\t" \
  "s_cmp_lg_u32 s71, 0\n\t" \
  "s_cbranch_scc1 .LH3sgk%=\n\t" \
  "buffer_load_dword v129, off, s[44:47], 0 sc1\n\t" \
  "s_waitcnt vmcnt(0)\n\t" \
  "v_readfirstlane_b32 s62, v129\n\t" \
  "s_cmp_lt_u32 s63, 0x100000\n\t" \
  "s_cbranch_scc1 .LH3sgk%=\n\t" \
  "s_mov_b32 s62, 1\n\t" \
  "v_mov_b32 v129, 1\n\t" \
  "buffer_store_dword v129, off, s[44:47], 0 sc1\n\t" \
  ".LH3sgk%=:\n\t" \
  "s_branch .LH3kvwait%=\n\t" \
  ".LH3tmwait%=:\n\t" \
  "buffer_load_dword v132, off, s[40:43], s75 sc1\n\t" \
  "s_waitcnt vmcnt(0)\n\t" \
  "v_readfirstlane_b32 s70, v132\n\t" \
  "s_cmp_lg_u32 s70, 0\n\t" \
  "s_cbranch_scc1 .LH3tmok%=\n\t" \
  "s_mov_b32 s70, s92\n\t" \
  "s_cmp_lg_u32 s62, 0\n\t" \
  "s_cbranch_scc1 .LH3tmok%=\n\t" \
  "s_add_u32 s63, s63, 1\n\t" \
  "s_and_b32 s71, s63, 255\n\t" \
  "s_cmp_lg_u32 s71, 0\n\t" \
  "s_cbranch_scc1 .LH3sgt%=\n\t" \
  "buffer_load_dword v129, off, s[44:47], 0 sc1\n\t" \
  "s_waitcnt vmcnt(0)\n\t" \
  "v_readfirstlane_b32 s62, v129\n\t" \
  "s_cmp_lt_u32 s63, 0x100000\n\t" \
  "s_cbranch_scc1 .LH3sgt%=\n\t" \
  "s_mov_b32 s62, 1\n\t" \
  "v_mov_b32 v129, 1\n\t" \
  "buffer_store_dword v129, off, s[44:47], 0 sc1\n\t" \
  ".LH3sgt%=:\n\t" \
  "s_branch .LH3tmwait%=\n\t" \
  ".LH3last%=:\n\t" \
  "ds_read_b64_tr_b16 v[8:9], v141 offset:3584\n\t" \
  "ds_read_b64_tr_b16 v[10:11], v141 offset:3840\n\t" \
  "s_waitcnt lgkmcnt(0)\n\t" \
  "buffer_store_dword v130, v150, s[40:43], s57 offen sc1\n\t" \
  "buffer_store_dwordx4 v[8:11], v151, s[40:43], s58 offen sc1\n\t" \
  "s_waitcnt vmcnt(0)\n\t" \
  "s_barrier\n\t" \
  ".LHend%=:\n\t"
template <bool ACC, int R, int O0, int O1, int O2, int O3> DI void tr4_to(unsigned base) {
  if constexpr (ACC)
    asm volatile("ds_read_b64_tr_b16 a[%c1:%c1+1], %0 offset:%c2\n\tds_read_b64_tr_b16 a[%c1+2:%c1+3], %0 offset:%c3\n\tds_read_b64_tr_b16 a[%c1+4:%c1+5], %0 offset:%c4\n\tds_read_b64_tr_b16 a[%c1+6:%c1+7], %0 offset:%c5"
                 :: "v"(base), "n"(R), "n"(O0), "n"(O1), "n"(O2), "n"(O3) : H_CLOB_W, "memory");
  else
    asm volatile("ds_read_b64_tr_b16 v[%c1:%c1+1], %0 offset:%c2\n\tds_read_b64_tr_b16 v[%c1+2:%c1+3], %0 offset:%c3\n\tds_read_b64_tr_b16 v[%c1+4:%c1+5], %0 offset:%c4\n\tds_read_b64_tr_b16 v[%c1+6:%c1+7], %0 offset:%c5"
                 :: "v"(base), "n"(R), "n"(O0), "n"(O1), "n"(O2), "n"(O3) : H_CLOB_W, "memory");
}
template <int R, int O0, int O1> DI void tr2_to(unsigned base) {
  asm volatile("ds_read_b64_tr_b16 v[%c1:%c1+1], %0 offset:%c2\n\tds_read_b64_tr_b16 v[%c1+2:%c1+3], %0 offset:%c3" :: "v"(base), "n"(R), "n"(O0), "n"(O1) : H_CLOB_W, "memory");
}

DI void role_h(const Params& p, ldsp lds, rsrc_t fl, rsrc_t ws, int xg, int y) {
  const int tid = threadIdx.x, lane = tid & 63, w = rfl(tid >> 6);
  const int h = xg * 8 + (y >> 2), eh = (y >> 1) & 1, ch = y & 1;
  const int prod = h * 2 + eh, unit = ch * 64 + prod;
  const int c16 = lane & 15, g = lane >> 4;
  const unsigned ldsb = (unsigned)(size_t)lds;
  bool dead = false;
  const u32x4 sent4 = {SENT, SENT, SENT, SENT};
  const unsigned pub_off = (unsigned)(g * 16384 + unit * 64 + (lane & 3) * 16 + ((lane >> 2) & 3) * 4);
  const unsigned unit_so = (unsigned)((unit >> 1) * 16384 + (unit & 1) * 4096 + w * 1024);

  STAMP(1000);
  const u32x4 pw = ld4(fl, FL_POL, 0);
  const unsigned pol = rfl((pw.x & 1) | ((pw.y & 1) << 1));
#pragma unroll
  for (int i = 0; i < 4; ++i) st4(sent4, ws, lane * 16, WS_PART + unit_so + (i & 1) * 8192 + (i >> 1) * MB);
#pragma unroll
  for (int i = 0; i < 2; ++i) st1(SENT, ws, WS_MEM + pub_off + i * 8192, w * 65536);

  if (blockIdx.x == 0) {
    const u32x4 z = {0u, 0u, 0u, 0u};
#pragma unroll
    for (int i = 0; i < 4; ++i) st4(z, ws, WS_TIME + (i * 4 + w) * 1024 + lane * 16, 0);
  }
  {
    const Rows256 R(lane, w, h, ldsb);
    unsigned touch = 0;
    const int q = (lane >> 2) & 3, pp = lane & 3;
    unsigned vv[2], ov[4], ord[4];
#pragma unroll
    for (int k = 0; k < 2; ++k) vv[k] = (unsigned)((32 * w + (lane >> 3)) * 8192 + h * 256 + eh * 128 + (((lane & 7) ^ (((lane >> 4) & 1) << 1) ^ (k << 2)) * 16));
    const unsigned vrd = ldsb + (8 * g + q) * 128 + (((2 * w + (pp >> 1)) ^ ((q >> 1) << 1) ^ ((g & 1) << 2)) * 16) + (pp & 1) * 8;
#pragma unroll
    for (int k = 0; k < 4; ++k) {
      ov[k] = (unsigned)((8 * w + 2 * k + (lane >> 5)) * 1024 + ch * 512 + (((lane & 31) ^ (((2 * k + (lane >> 5)) & 7) << 1)) * 16));
      ord[k] = ldsb + (4 * g + q) * 512 + (((8 * w + 2 * k + (pp >> 1)) ^ (((4 * g + q) & 7) << 1)) * 16) + (pp & 1) * 8;
    }
    auto issue = [&](int i) {
      const unsigned dst = rfl(ldsb + (i & 7) * 16384 + w * 4096);
      if (i < 16) R.issue(i < 8 ? p.wq : p.wk, i & 7, dst);
      else if (i < 20) {
#pragma unroll
        for (int k = 0; k < 4; ++k) glds16s((const unsigned char*)p.wv + (size_t)((i - 16) * 128 + 8 * k) * 8192, vv[k & 1], dst + k * 1024);
      } else {
#pragma unroll
        for (int k = 0; k < 4; ++k) glds16s((const unsigned char*)p.wo + (size_t)(h * 128 + eh * 64 + (i - 20) * 32) * 1024, ov[k], dst + k * 1024);
        if (i == 21) touch = ld1(ws, (w == 3 && lane < 3) ? (unsigned)(y * 3 + lane) * 2 * MB : OOB, 0);
      }
    };
    auto consume = [&](auto ic) {
      constexpr int I = decltype(ic)::value, B = (I & 7) * 16384;
      if constexpr (I == 0) { if (tid == 0) st1(1u, fl, FL_INIT + (xg * 32 + y) * 4, 0); }
      if constexpr (I < 16) {
        tr4_to<true, (I >> 3) * 128 + (I & 7) * 8, B & 0xffff, (B & 0xffff) + 4 * 256, (B & 0xffff) + 32 * 256, (B & 0xffff) + 36 * 256>(R.rd[0] + (B & ~0xffff));
        tr4_to<true, (I >> 3) * 128 + 64 + (I & 7) * 8, B & 0xffff, (B & 0xffff) + 4 * 256, (B & 0xffff) + 32 * 256, (B & 0xffff) + 36 * 256>(R.rd[1] + (B & ~0xffff));
      } else if constexpr (I < 20) {
        tr4_to<false, 160 + (I - 16) * 16, B, B + 4 * 128, B + 32 * 128, B + 36 * 128>(vrd);
        tr4_to<false, 168 + (I - 16) * 16, B + 8192, B + 8192 + 4 * 128, B + 8192 + 32 * 128, B + 8192 + 36 * 128>(vrd);
      } else {
        tr2_to<224 + (I - 20) * 4, 0, 8192>(ord[0] + B); tr2_to<232 + (I - 20) * 4, 0, 8192>(ord[1] + B);
        tr2_to<240 + (I - 20) * 4, 0, 8192>(ord[2] + B); tr2_to<248 + (I - 20) * 4, 0, 8192>(ord[3] + B);
      }
    };
    load_blocks<0, 22, 22>(issue, consume);
    asm volatile("" :: "v"(touch));
  }
  const unsigned dt1 = RT(); (void)dt1;
  (void)wait_flags(fl, FL_INIT + lane * 4, 1u, dead);
  (void)wait_flags(fl, FL_INIT + 256 + lane * 4, 1u, dead);
  constexpr int L_QK = 8192, L_VM = 12800, L_RED = 14848, L_KV0 = 16384, L_KV1 = 32768;
  unsigned kv_so[3];
#pragma unroll
  for (int i = 0; i < 3; ++i) { const int f = 3 * w + i; kv_so[i] = (f < 8 ? WS_KX + f * 1024 : WS_VX + (4 * eh + f - 8) * 1024) + h * 16384; }
  const unsigned far = ~pol;
  const unsigned kvflag = FL_KV + h * 256;
  BAR();
  if (w == 0) {
    const unsigned one = (lane & 1) ? 0x3F800000u : 0x00003F80u, d = (lane & 7) >> 1;
    const u32x4 sv = {d == 0 ? one : 0u, d == 1 ? one : 0u, d == 2 ? one : 0u, d == 3 ? one : 0u};
    *(LAS u32x4*)(lds + 15360 + lane * 16) = sv;
  }
  (void)wait_flags(fl, kvflag + (lane & 1) * 128, 1u, dead);
  {
    u32x4 t[6];
#pragma unroll
    for (int i = 0; i < 6; ++i) t[i] = ld4(ws, lane * 16, kv_so[i % 3] + blk(i / 3, 0, far));
#pragma unroll
    for (int i = 0; i < 6; ++i) *(LAS u32x4*)(lds + (i < 3 ? L_KV0 : L_KV1) + (3 * w + i % 3) * 1024 + lane * 16) = t[i];
  }
  BAR();
  const unsigned dt2 = RT(), dc2 = CT(); (void)dt2; (void)dc2;
  {
    const unsigned l16 = lane * 16;
    const uint16_t* m0 = p.mem0 + (c16 & 7) * 512 + 128 * w + 16 * g + (c16 >> 3) * 2;
    const unsigned xoff = (ch == 0 && lane == 0) ? (unsigned)(FL_MD + prod * 128) : OOB;
    asm volatile("v_mov_b32 v138, %0\n\tv_mov_b32 v139, %1\n\tv_mov_b32 v140, %2\n\tv_mov_b32 v141, %3\n\tv_mov_b32 v142, %4\n\tv_mov_b32 v143, %5\n\tv_mov_b32 v144, %6\n\t"
                 "v_mov_b32 v145, %7\n\tv_mov_b32 v146, %8\n\tv_mov_b32 v147, %9\n\tv_mov_b32 v148, %10\n\tv_mov_b32 v149, %11\n\tv_mov_b32 v150, %12\n\tv_mov_b32 v151, %13\n\t"
                 "v_mov_b32 v152, %14\n\tv_mov_b64 v[128:129], %15\n\tv_mov_b32 v153, %16"
                 :: "v"((lane & 8) ? 0xEEEE : 0x4444), "v"(l16),
                    "v"(L_QK + (32 * w + 16 * (lane >> 5) + c16) * 16 + (g & 1) * 8), "v"(L_QK + (4 * g + ((lane >> 2) & 3)) * 16 + (lane & 1) * 8),
                    "v"(L_VM + lane * 32 + w * 8), "v"(L_VM + lane * 32),
                    "v"(L_RED + (g * 8 + (lane & 7)) * 16 + w * 4), "v"(L_RED + ((((lane >> 2) & 1) * 2 + ((lane >> 3) & 1)) * 8 + (lane & 3)) * 16),
                    "v"(L_KV0 + l16), "v"(L_KV1 + l16),
                    "v"((unsigned)((ch * 32 + 2 * (4 * w + (lane >> 5)) + ((c16 >> 1) & 1)) * 16384 + (g & 1) * 4096 + prod * 64 + ((c16 >> 2) * 2 + (c16 & 1)) * 8)),
                    "v"(pub_off), "v"(ch == 0 ? (unsigned)(lane * 4) : OOB), "v"((ch == 0 && eh == 0) ? l16 : OOB), "v"(xoff), "v"(m0), "v"((blockIdx.x == 0 && lane == 0) ? 0u : OOB)
                 : H_CLOB_W, H_CLOB_AD);
    int sel = -1; unsigned pfm = p.t0, xo = p.pen;
    asm volatile("s_mov_b64 s[40:41], %0\n\ts_mov_b32 s42, %1\n\ts_mov_b32 s43, 0x20000\n\ts_mov_b64 s[44:45], %2\n\ts_mov_b32 s46, %3\n\ts_mov_b32 s47, 0x20000\n\t"
                 "s_mov_b32 s48, %4\n\ts_mov_b32 s49, %5\n\ts_mov_b32 s50, %6\n\ts_mov_b32 s51, %7\n\ts_mov_b32 s52, %8\n\ts_mov_b32 s53, %9\n\ts_mov_b32 s54, %10\n\ts_mov_b32 s55, %11\n\t"
                 "s_mov_b32 s56, %12\n\ts_mov_b32 s57, %13\n\ts_mov_b32 s58, %14\n\ts_mov_b32 s59, %15\n\ts_mov_b32 s60, %16\n\ts_mov_b32 s61, 0\n\ts_mov_b32 s62, %17\n\ts_mov_b32 s63, 0\n\t"
                 "s_mov_b32 s76, %18\n\ts_mov_b32 s77, %18\n\ts_mov_b32 s78, %19\n\ts_mov_b32 s79, 0x42700000\n\ts_mov_b32 s80, 0xffff0000\n\ts_mov_b32 s81, %20\n\ts_mov_b32 s82, %21\n\t"
                 "s_mov_b32 s83, 0\n\ts_mov_b32 s84, 0\n\ts_mov_b32 s85, %22\n\ts_mov_b32 s86, 0\n\ts_mov_b32 s88, %23\n\ts_mov_b32 s89, %24\n\ts_mov_b32 s90, %25\n\ts_mov_b32 s93, %26\n\t"
                 H_LOOP_ASM
                 :: "s"(p.data), "n"(WS_END), "s"(p.flags), "n"(FL_BYTES),
                    "s"(WS_PART + (pol & 1) * 8192), "s"(WS_PART + MB + (pol & 1) * 8192), "s"(WS_MEM + ((pol >> 1) & 1) * 8192),
                    "s"(unit_so), "s"((unsigned)(xg * 16384 + w * 2048)),
                    "s"(kv_so[0] + blk(2, 0, far)), "s"(kv_so[1] + blk(2, 0, far)), "s"(kv_so[2] + blk(2, 0, far)), "s"(kvflag),
                    "s"(WS_VM + blk(0, h, far) + (4 * eh + w) * 256), "s"(WS_KM + blk(0, h, far) + w * 1024), "s"(p.zb0), "s"(p.d2), "s"(rfl(dead ? 1u : 0u)),
                    "s"(p.cap), "n"(__builtin_bit_cast(unsigned, SARG)), "n"(__builtin_bit_cast(unsigned, SCALE * LOG2E)), "s"(w), "s"(sel), "s"(pfm), "s"(xo), "s"(WS_TIME + ((pol >> 1) & 1) * 8192), "s"(p.d1)
                 : H_CLOB_W, H_CLOB_AD, H_CLOB_V, H_CLOB_S, "vcc", "scc", "memory");
  }
  DBGV(1001, dt1); DBGV(1002, dt2); DBGV(1007, dc2); STAMP(1003); DBGV(1008, mtime()); DBGV(1004, nfa); DBGV(1005, nfb); DBGV(1006, pol); DBGV(1009, ptsum); DBGV(1011, zbf); DBGV(1010, (unsigned)__builtin_amdgcn_s_getreg(20 | (3 << 11)) & 0xfu);
  DBG_DUMP();
}

DI void role_w(const Params& p, ldsp lds, rsrc_t fl, rsrc_t ws, int xg, int y) {
  const int tid = threadIdx.x, lane = tid & 63, w = rfl(tid >> 6);
  const int h = xg * 8 + (y >> 1), par = y & 1;
  const int c16 = lane & 15, g = lane >> 4;
  const unsigned ldsb = (unsigned)(size_t)lds;
  bool dead = false;
  const unsigned far = rfl(~ld1(fl, FL_POL, 0));
  STAMPG(1000);
  constexpr int L_P = 81920;
  constexpr int LEAD = 4;
  bf16x8 wq[2][16], wk[2][16], wv[2][16];
  const unsigned xvo = (unsigned)((((w >> 1) * 16 + c16) * 512 + (w & 1) * 256 + g * 8) * 2);
  auto x_piece = [&](int n, int img, int i) {
    glds16s((const unsigned char*)p.x + (size_t)(2 * n + par) * 32768 + i * 64, xvo, rfl(ldsb + (img ? 98304 : 131072) + w * 8192 + i * 1024));
  };
  auto p1 = [&](auto kvc, auto qc, int n, int slot, auto&& hook) {
    constexpr bool KV = decltype(kvc)::value, Q = decltype(qc)::value;
    f32x4 cq[2][2], ck[2][2], cv[2][2];
    const ldsp xb = lds + ((n & 1) ? 98304 : 131072) + lane * 16;
    bf16x8 xf[2][2];
    xf[0][0] = asb8(lds4(xb)); xf[0][1] = asb8(lds4(xb + 16384));
#pragma unroll
    for (int kd = 0; kd < 16; ++kd) {
      if (kd + 1 < 16) { xf[(kd + 1) & 1][0] = asb8(lds4(xb + (kd + 1) * 1024)); xf[(kd + 1) & 1][1] = asb8(lds4(xb + 16384 + (kd + 1) * 1024)); }
      hook(kd);
#pragma unroll
      for (int t = 0; t < 2; ++t)
#pragma unroll
        for (int tt = 0; tt < 2; ++tt) {
          if (kd == 0) {
            if constexpr (KV) { MF0_AV(ck[t][tt], wk[t][kd], xf[0][tt]); MF0(cv[t][tt], xf[0][tt], wv[t][kd]); }
            if constexpr (Q) MF0_AV(cq[t][tt], wq[t][kd], xf[0][tt]);
          } else {
            if constexpr (KV) { MF_AV(ck[t][tt], wk[t][kd], xf[kd & 1][tt]); MF(cv[t][tt], xf[kd & 1][tt], wv[t][kd]); }
            if constexpr (Q) MF_AV(cq[t][tt], wq[t][kd], xf[kd & 1][tt]);
          }
        }
    }
    NOPS(8);
    if constexpr (KV) { USE4(ck[0][0], ck[0][1], ck[1][0], ck[1][1]); USE4(cv[0][0], cv[0][1], cv[1][0], cv[1][1]); }
    if constexpr (Q) USE4(cq[0][0], cq[0][1], cq[1][0], cq[1][1]);
    hook(16);
    const unsigned sb = blk(2 * n + par, h, far);
    const ldsp qk = lds + slot * 16384 + w * 1024 + lane * 16;
#pragma unroll
    for (int u = 0; u < 2; ++u) {
      if constexpr (KV) {
        u32x4 tk = {cvtpk(ck[0][u][0], ck[0][u][1]), cvtpk(ck[0][u][2], ck[0][u][3]), cvtpk(ck[1][u][0], ck[1][u][1]), cvtpk(ck[1][u][2], ck[1][u][3])};
        st4(tk, ws, lane * 16, WS_KX + sb + (u * 4 + w) * 1024);
        if constexpr (Q) *(LAS u32x4*)(qk + 8192 + u * 4096) = tk;
        u32x4 tv = {cvtpk(cv[u][0][0], cv[u][0][1]), cvtpk(cv[u][0][2], cv[u][0][3]), cvtpk(cv[u][1][0], cv[u][1][1]), cvtpk(cv[u][1][2], cv[u][1][3])};
        st4(tv, ws, lane * 16, WS_VX + sb + (2 * w + u) * 1024);
      }
      if constexpr (Q) {
        u32x4 tq = {cvtpk(cq[0][u][0], cq[0][u][1]), cvtpk(cq[0][u][2], cq[0][u][3]), cvtpk(cq[1][u][0], cq[1][u][1]), cvtpk(cq[1][u][2], cq[1][u][3])};
        *(LAS u32x4*)(qk + u * 4096) = tq;
        if constexpr (!KV) *(LAS u32x4*)(qk + 8192 + u * 4096) = ld4(ws, lane * 16, WS_KX + sb + (u * 4 + w) * 1024);
      }
    }
  };
  const unsigned kvf = FL_KV + (h * 2 + par) * 128, odf = FL_OD + (par * 32 + h) * 128;
  constexpr std::true_type YES{}; constexpr std::false_type NO{};
  {
    const Rows256 R(lane, w, h, ldsb);
    auto issue = [&](int i) {
      R.issue(i < 8 ? p.wk : i < 16 ? p.wv : p.wq, i & 7, rfl(ldsb + (i & 7) * 16384 + w * 4096));
      if (i == 15) {
#pragma unroll
        for (int k = 0; k < 8; ++k) x_piece(0, 0, k);
      }
    };
    auto take = [&](auto ic, auto sc) {
      constexpr int I = decltype(ic)::value, J = (I & 7) * 2, B = decltype(sc)::value * 16384, BL = B & 0xffff;
      bf16x16 t0, t1;
      const unsigned b0 = R.rd[0] + (B & ~0xffff), b1 = R.rd[1] + (B & ~0xffff);
      if constexpr (I < 8) tr8a<BL, BL + 4 * 256, BL + 32 * 256, BL + 36 * 256>(b0, b1, wk[0][J], wk[0][J + 1], wk[1][J], wk[1][J + 1]);
      else if constexpr (I >= 16) tr8a<BL, BL + 4 * 256, BL + 32 * 256, BL + 36 * 256>(b0, b1, wq[0][J], wq[0][J + 1], wq[1][J], wq[1][J + 1]);
      else { tr8<BL, BL + 4 * 256, BL + 32 * 256, BL + 36 * 256>(b0, b1, t0, t1); wv[0][J] = lo8(t0); wv[0][J + 1] = hi8(t0); wv[1][J] = lo8(t1); wv[1][J + 1] = hi8(t1); }
    };
    load_blocks<0, 16, 22>(issue, [&](auto ic) { take(ic, std::integral_constant<int, decltype(ic)::value & 7>{}); });
    STAMPG(1001);
    vmwait<24>();
    BAR();
#pragma unroll
    for (int i = 0; i < 8; ++i) x_piece(1, 1, i);
#pragma nounroll
    for (int n = 0; n < 2; ++n) {
      p1(YES, NO, n, 0, [&](int kd) { if (kd == 3 && n == 1) { VM0(); asm volatile("s_barrier" ::: "memory"); if (tid == 0) st1(1u, fl, kvf, 0); } });
      if (n == 0) { vmwait<4>(); BAR(); }
    }
    STAMPG(1002);
    take(std::integral_constant<int, 16>{}, std::integral_constant<int, 0>{}); take(std::integral_constant<int, 17>{}, std::integral_constant<int, 1>{});
    BAR();
    R.issue(p.wq, 6, rfl(ldsb + w * 4096)); R.issue(p.wq, 7, rfl(ldsb + 16384 + w * 4096));
    take(std::integral_constant<int, 18>{}, std::integral_constant<int, 2>{}); take(std::integral_constant<int, 19>{}, std::integral_constant<int, 3>{});
    vmwait<8>();
    BAR();
    if (tid == 0) st1(2u, fl, kvf, 0);
    take(std::integral_constant<int, 20>{}, std::integral_constant<int, 4>{}); take(std::integral_constant<int, 21>{}, std::integral_constant<int, 5>{});
    VM0();
    BAR();
    take(std::integral_constant<int, 22>{}, std::integral_constant<int, 0>{}); take(std::integral_constant<int, 23>{}, std::integral_constant<int, 1>{});
    BAR();
#pragma nounroll
    for (int n = 0; n < 2; ++n) {
      p1(NO, YES, n, n, [&](int kd) { if (n == 1 && kd < 8) x_piece(2, 0, kd); });
      BAR();
    }
    VM0();
  }
  int slot = 2;
  for (int it = 2; it < 32 + LEAD; ++it) {
    STAMPG(it * 8);
    const unsigned mdo = FL_MD + (h * 2 + (lane & 1)) * 128;
    const int seg = 2 * (it - LEAD) + par, tt = w & 1;
    const unsigned sb = blk(seg, h, far);
    unsigned md = 0;
    bf16x8 vx[2], km[4];
    unsigned vm1[2];
    auto p2_loads = [&]() {
      if (!__all(md > (unsigned)seg)) (void)wait_flags(fl, mdo, (unsigned)seg + 1u, dead);
      STAMPG(it * 8 + 1);
#pragma unroll
      for (int ks = 0; ks < 4; ++ks) km[ks] = asb8(ld4(ws, lane * 16, WS_KM + sb + ks * 1024));
#pragma unroll
      for (int t = 0; t < 2; ++t) { vx[t] = asb8(ld4(ws, lane * 16, WS_VX + sb + (2 * w + t) * 1024)); vm1[t] = ld1(ws, lane * 4, WS_VM + sb + (2 * w + t) * 256); }
    };
    bool early = false;
    if (it < 32) {
      if (it <= LEAD) vmwait<4>(); else vmwait<3>();
      BAR();
      const int nx = it + 1 < 32 ? it + 1 : it;
      p1(YES, YES, it, slot, [&](int kd) {
        if (kd < 8) x_piece(nx, (it + 1) & 1, kd);
        if (kd == 6) {
          vmwait<7>();
          asm volatile("s_barrier" ::: "memory");
          if (tid == 0) { if (it > LEAD) st1((unsigned)(it - LEAD), fl, odf, 0); else st1((unsigned)it, fl, kvf, 0); }
        }
        if (kd == 11) md = ld1(fl, mdo, 0);
        if (kd == 16 && it >= LEAD && __all(md > (unsigned)seg)) { early = true; p2_loads(); }
      });
    } else {
      VM0();
      BAR();
      if (tid == 0) st1((unsigned)(it - LEAD), fl, odf, 0);
    }
    slot = slot == 4 ? 0 : slot + 1;
    STAMPG(it * 8 + 2);
    if (it >= LEAD) {
      if (!early) p2_loads();
      bf16x8 qf[4], kx[2][4];
      const ldsp qk = lds + slot * 16384 + lane * 16;
#pragma unroll
      for (int ks = 0; ks < 4; ++ks) {
        qf[ks] = asb8(lds4(qk + (tt * 4 + ks) * 1024));
        kx[0][ks] = asb8(lds4(qk + 8192 + ks * 1024));
        kx[1][ks] = asb8(lds4(qk + 8192 + (4 * tt + ks) * 1024));
      }
      f32x4 sx[2], sm;
      MF0(sx[0], kx[0][0], qf[0]); MF0(sx[1], kx[1][0], qf[0]);
#pragma unroll
      for (int ks = 1; ks < 4; ++ks) { MF(sx[0], kx[0][ks], qf[ks]); MF(sx[1], kx[1][ks], qf[ks]); }
      NOPS(8);
      USE2(sx[0], sx[1]);
      const float NEG = rbf(-1000000000.0f) * LOG2E;
      float sc[10];
#pragma unroll
      for (int i = 0; i < 8; ++i) { const int key = (i >> 2) * 16 + 4 * g + (i & 3); sc[i] = key <= tt * 16 + c16 ? sx[i >> 2][i & 3] * (SCALE * LOG2E) : NEG; }
      USE4(sc[0], sc[1], sc[2], sc[3]); USE4(sc[4], sc[5], sc[6], sc[7]);
      MF0(sm, km[0], qf[0]); MF(sm, km[1], qf[1]); MF(sm, km[2], qf[2]); MF(sm, km[3], qf[3]);
      NOPS(8);
      USE1(sm);
      sc[8] = sel_l32(sm[0], sm[1]) * (SCALE * LOG2E); sc[9] = sel_l32(sm[2], sm[3]) * (SCALE * LOG2E);
      const float m = allmax_g(fmx3(fmx3(sc[0], sc[1], sc[2]), fmx3(sc[3], sc[4], sc[5]), fmx(fmx3(sc[6], sc[7], sc[8]), sc[9])));
      float l = 0.f;
#pragma unroll
      for (int i = 0; i < 10; ++i) { sc[i] = ex2(sc[i] - m); l += sc[i]; }
      const float inv = __builtin_amdgcn_rcpf(allsum_g(l));
      u32x4 px = {cvtpk(sc[0] * inv, sc[1] * inv), cvtpk(sc[2] * inv, sc[3] * inv), cvtpk(sc[4] * inv, sc[5] * inv), cvtpk(sc[6] * inv, sc[7] * inv)};
      u32x2 pm = {cvtpk(sc[8] * inv, sc[9] * inv), 0u};
      if (w < 2) { *(LAS u32x4*)(lds + L_P + w * 2048 + lane * 16) = px; *(LAS unsigned*)(lds + L_P + w * 2048 + 1024 + lane * 4) = pm.x; }
      u32x2 vm[2] = {{vm1[0], 0u}, {vm1[1], 0u}};
      USE4(vm[0], vm[1], vx[0], vx[1]);
      VM0();
      BAR();
      if (it < 32 && tid == 0) st1((unsigned)it + 1u, fl, kvf, 0);
      u32x4 px2 = lds4(lds + L_P + (tt ^ 1) * 2048 + lane * 16);
      u32x2 pm2 = {*(LAS unsigned*)(lds + L_P + (tt ^ 1) * 2048 + 1024 + lane * 4), 0u};
      USE4(px, pm, px2, pm2); USE2(vm[0], vm[1]);
      NOPS(1);
      f32x4 o[2][2];
      MF0(o[0][0], vx[0], px); MF0(o[0][1], vx[1], px); MF0(o[1][0], vx[0], px2); MF0(o[1][1], vx[1], px2);
      MFH(o[0][0], vm[0], pm); MFH(o[0][1], vm[1], pm); MFH(o[1][0], vm[0], pm2); MFH(o[1][1], vm[1], pm2);
      NOPS(8);
      USE4(o[0][0], o[0][1], o[1][0], o[1][1]);
#pragma unroll
      for (int u = 0; u < 2; ++u) {
        u32x4 t4 = {cvtpk(o[u][0][0], o[u][0][1]), cvtpk(o[u][0][2], o[u][0][3]), cvtpk(o[u][1][0], o[u][1][1]), cvtpk(o[u][1][2], o[u][1][3])};
        st4(t4, ws, lane * 16, WS_OT + sb + ((tt ^ u) * 4 + w) * 1024);
      }
    }
  }
  VM0();
  BAR();
  if (tid == 0) st1(32u, fl, odf, 0);
  STAMPG(1003);
}

DI float wave_sum(float v) {
#pragma unroll
  for (int m = 1; m < 64; m <<= 1) v += __shfl_xor(v, m);
  return v;
}
DI float sigm(float x) { return 1.0f / (1.0f + __expf(-x)); }
DI void role_t(const Params& p, ldsp lds, rsrc_t fl, rsrc_t ws, int xg, int y) {
  const int tid = threadIdx.x, lane = tid & 63, w = rfl(tid >> 6);
  const int slice = y - 16, phase = xg >> 1, tile = xg & 1, tix = xg * 16 + slice;
  const unsigned tstart = mtime(), far = rfl(~ld1(fl, FL_POL, 0));
  const int c16 = lane & 15, g = lane >> 4;
  const unsigned ldsb = (unsigned)(size_t)lds;
  bool dead = false;
  constexpr int L_TOP = L_MISC + 64, L_IDX = L_TOP + 128, L_RS = L_IDX + 32, L_Y = L_MISC + 512, L_F = L_MISC + 2048;
  STAMP(1000);
  { const unsigned touch = ld1(ws, (w == 3 && lane < 6) ? (unsigned)(slice * 6 + lane) * 2 * MB : OOB, 0); asm volatile("" :: "v"(touch)); }
  {
    const u32x4 wr = *(const u32x4*)(p.wr + lane * 8), gw = *(const u32x4*)(p.gw + lane * 8);
    float rv = 0.f, gv = 0.f;
    for (int t = 0; t < 8; ++t) {
      const u32x4 xv = *(const u32x4*)(p.x + (size_t)(tix * 32 + w * 8 + t) * 512 + lane * 8);
      float r = 0.f, s = 0.f;
#pragma unroll
      for (int i = 0; i < 4; ++i) {
        r = fmaf(bflo(xv[i]), bflo(wr[i]), r); r = fmaf(bfhi(xv[i]), bfhi(wr[i]), r);
        const unsigned pr = cvtpk(bflo(xv[i]) * bflo(gw[i]), bfhi(xv[i]) * bfhi(gw[i]));
        s += bflo(pr); s += bfhi(pr);
      }
      r = wave_sum(r); s = wave_sum(s);
      if (lane == t) { rv = rbf(r); gv = rbf(sigm(rbf(s))); }
    }
    if (lane < 8) { st1(f2u(rv), ws, (tix * 32 + w * 8 + lane) * 4, WS_R); st1(f2u(gv), ws, (tix * 32 + w * 8 + lane) * 4, WS_G); }
    VM0();
    BAR();
    if (tid == 0) st1(1u, fl, FL_RDONE + tix * 4, 0);
  }
  const unsigned ring = rfl(ldsb + w * 32768);
  const unsigned nb = ring + (8 * g + ((lane >> 2) & 3)) * 64 + (lane & 3) * 8;
  const unsigned pb = ring + (4 * g + ((lane >> 2) & 3)) * 64 + (lane & 3) * 8;
  {
    bf16x8 w1[2][8], w2[2][14];
    const int fcol = tix < 56 ? tix * 32 : 0;
    const unsigned char* s1 = (const unsigned char*)((w & 1) ? p.wu : p.wg) + ((size_t)((w >> 1) * 256 + (lane >> 2)) * FF + fcol) * 2 + (lane & 3) * 16;
    const unsigned char* s2 = (const unsigned char*)p.wd + ((size_t)(w * 448 + (lane >> 2)) * 512 + slice * 32) * 2 + (lane & 3) * 16;
    auto issue = [&](int i) {
      if (i < 4) slot_issue(s1 + (size_t)i * 64 * FF * 2, FF * 2, ring + (i & 7) * 4096);
      else slot_issue(s2 + (size_t)(i - 4) * 64 * 1024, 1024, ring + (i & 7) * 4096);
    };
    auto consume = [&](auto ic) {
      constexpr int I = decltype(ic)::value;
      const bf16x16 t0 = frag_nat<I, 0>(nb), t1 = frag_nat<I, 1>(nb);
      if constexpr (I < 4) { w1[0][2 * I] = lo8(t0); w1[0][2 * I + 1] = hi8(t0); w1[1][2 * I] = lo8(t1); w1[1][2 * I + 1] = hi8(t1); }
      else { w2[0][2 * I - 8] = lo8(t0); w2[0][2 * I - 7] = hi8(t0); w2[1][2 * I - 8] = lo8(t1); w2[1][2 * I - 7] = hi8(t1); }
    };
    while ((int)(mtime() - tstart) < p.tdel) __builtin_amdgcn_s_sleep(8);
    load_stream<0, 11>(issue, consume);
    (void)wait_flags(fl, FL_RDONE + lane * 4, 1u, dead);
    unsigned key[8];
#pragma unroll
    for (int i = 0; i < 2; ++i) {
      const u32x4 rv = ld4(ws, (w * 512 + i * 256 + lane * 4) * 4, WS_R);
#pragma unroll
      for (int k = 0; k < 4; ++k) {
        const unsigned b = rv[k] >> 16, o = (b & 0x7fffu) > 0x7f80u ? 0u : ((b & 0x8000u) ? (~b & 0xffffu) : (b | 0x8000u));
        const unsigned oz = (b & 0x7fffu) == 0u ? 0x8000u : o;
        key[i * 4 + k] = (oz << 16) | (0xffffu - (unsigned)(w * 512 + i * 256 + lane * 4 + k));
      }
    }
    for (int r = 0; r < 8; ++r) {
      unsigned m = key[0];
#pragma unroll
      for (int i = 1; i < 8; ++i) m = max(m, key[i]);
#pragma unroll
      for (int d = 1; d < 64; d <<= 1) m = max(m, (unsigned)__shfl_xor((int)m, d));
#pragma unroll
      for (int i = 0; i < 8; ++i) key[i] = key[i] == m ? 0u : key[i];
      if (lane == 0) *(LAS unsigned*)(lds + L_TOP + (w * 8 + r) * 4) = m;
    }
    BAR();
    if (w == 0 && lane < 32) {
      const unsigned mine = *(LAS unsigned*)(lds + L_TOP + lane * 4);
      int rank = 0;
      for (int i = 0; i < 32; ++i) rank += *(LAS unsigned*)(lds + L_TOP + i * 4) > mine;
      if (rank < 8) {
        const unsigned idx = 0xffffu - (mine & 0xffffu);
        *(LAS unsigned*)(lds + L_IDX + rank * 4) = idx;
        *(LAS float*)(lds + L_RS + rank * 4) = rbf(sigm(u2f(ld1(ws, idx * 4, WS_R))));
      }
    }
    BAR();
    {
      const unsigned idx = *(LAS unsigned*)(lds + L_IDX + (c16 & 7) * 4);
      const uint16_t* xr = p.x + (size_t)idx * 512 + (w >> 1) * 256 + g * 8;
      f32x4 c[2];
      bf16x8 xa[8];
#pragma unroll
      for (int kd = 0; kd < 8; ++kd) xa[kd] = asb8(*(const u32x4*)(xr + kd * 32));
#pragma unroll
      for (int kd = 0; kd < 8; ++kd)
#pragma unroll
        for (int t = 0; t < 2; ++t) { if (kd == 0) MF0(c[t], xa[kd], w1[t][kd]); else MF(c[t], xa[kd], w1[t][kd]); }
      NOPS(8);
      USE2(c[0], c[1]);
#pragma unroll
      for (int t = 0; t < 2; ++t) *(LAS f32x4*)(lds + L_F + (w * 2 + t) * 1024 + lane * 16) = c[t];
      BAR();
      if (tix < 56) {
        const int row = tid >> 5, col = tid & 31;
        const int o = (col >> 4) * 1024 + ((col & 15) + 16 * (row >> 2)) * 16 + (row & 3) * 4;
        const float gp = rbf(*(LAS float*)(lds + L_F + o) + *(LAS float*)(lds + L_F + 4096 + o));
        const float up = rbf(*(LAS float*)(lds + L_F + 2048 + o) + *(LAS float*)(lds + L_F + 6144 + o));
        const float hv = rbf(gp * sigm(gp)) * up;
        const float nbv = u2f(__builtin_amdgcn_update_dpp(0, (int)f2u(hv), 0xB1, 0xf, 0xf, false));
        if ((tid & 1) == 0) st1(cvtpk(hv, nbv), ws, (row * FF + fcol + col) * 2, WS_HB);
      }
      VM0();
      BAR();
      if (tid == 0 && tix < 56) st1(1u, fl, FL_HDONE + tix * 4, 0);
    }
    {
      (void)wait_flags(fl, FL_HDONE + (lane < 56 ? lane : 0) * 4, 1u, dead);
      f32x4 c[2];
      bf16x8 ha[14];
#pragma unroll
      for (int kd = 0; kd < 14; ++kd) ha[kd] = asb8(ld4(ws, ((c16 & 7) * FF + w * 448 + kd * 32 + g * 8) * 2, WS_HB));
#pragma unroll
      for (int kd = 0; kd < 14; ++kd)
#pragma unroll
        for (int t = 0; t < 2; ++t) { if (kd == 0) MF0(c[t], ha[kd], w2[t][kd]); else MF(c[t], ha[kd], w2[t][kd]); }
      NOPS(8);
      USE2(c[0], c[1]);
#pragma unroll
      for (int t = 0; t < 2; ++t) *(LAS f32x4*)(lds + L_F + (w * 2 + t) * 1024 + lane * 16) = c[t];
      BAR();
      const int row = tid >> 5, col = tid & 31;
      const int o = (col >> 4) * 1024 + ((col & 15) + 16 * (row >> 2)) * 16 + (row & 3) * 4;
      const float yv = rbf((*(LAS float*)(lds + L_F + o) + *(LAS float*)(lds + L_F + 2048 + o)) + (*(LAS float*)(lds + L_F + 4096 + o) + *(LAS float*)(lds + L_F + 6144 + o)));
      *(LAS float*)(lds + L_Y + tid * 4) = rbf(yv * *(LAS float*)(lds + L_RS + row * 4));
    }
  }
  STAMP(1001);
  bf16x8 wo[2][32];
  {
    const unsigned char* s3 = (const unsigned char*)p.wo + ((size_t)(w * 1024 + (lane >> 2)) * 512 + slice * 32) * 2 + (lane & 3) * 16;
    auto issue = [&](int i) { slot_issue(s3 + (size_t)i * 64 * 1024, 1024, ring + (i & 7) * 4096); };
    auto consume = [&](auto ic) {
      constexpr int I = decltype(ic)::value;
      const bf16x16 t0 = frag_pi<I, 0>(pb), t1 = frag_pi<I, 1>(pb);
      wo[0][2 * I] = lo8(t0); wo[0][2 * I + 1] = hi8(t0); wo[1][2 * I] = lo8(t1); wo[1][2 * I + 1] = hi8(t1);
      TO_A(wo[0][2 * I]); TO_A(wo[0][2 * I + 1]); TO_A(wo[1][2 * I]); TO_A(wo[1][2 * I + 1]);
    };
    load_stream<0, 16>(issue, consume);
  }
  BAR();
  constexpr int L_C = 0;
  for (int seg = phase; seg < NSEG; seg += 2) {
    STAMP((seg >> 1) * 8);
    const int tok = tid >> 4, cc = (tid & 15) * 2;
    const int gtok = seg * 32 + tile * 16 + tok;
    const unsigned xv = *(const unsigned*)(p.x + (size_t)gtok * 512 + slice * 32 + cc);
    const float gt = u2f(ld1(ws, gtok * 4, WS_G));
    (void)wait_flags(fl, FL_OD + ((seg & 1) * 32 + (lane & 31)) * 128, (unsigned)(seg >> 1) + 1u, dead);
    STAMP((seg >> 1) * 8 + 1);
    f32x4 c[2];
    const unsigned ob = WS_OT + blk(seg, w * 8, far) + tile * 4096;
    u32x4 of[32];
#pragma unroll
    for (int i = 0; i < 32; ++i) of[i] = ld4(ws, lane * 16 + (i & 3) * 1024, ob + (i >> 2) * 16384);
#pragma unroll
    for (int i = 0; i < 32; ++i)
#pragma unroll
      for (int t = 0; t < 2; ++t) { if (i == 0) MF0_VA(c[t], of[0], wo[t][0]); else MF_VA(c[t], of[i], wo[t][i]); }
    NOPS(8);
    USE2(c[0], c[1]);
#pragma unroll
    for (int t = 0; t < 2; ++t)
#pragma unroll
      for (int i = 0; i < 4; ++i) *(LAS float*)(lds + L_C + ((w * 16 + 4 * g + i) * 36 + t * 16 + c16) * 4) = c[t][i];
    BAR();
    f32x2 av = *(LAS f32x2*)(lds + L_C + (tok * 36 + cc) * 4);
#pragma unroll
    for (int u = 1; u < 4; ++u) av += *(LAS f32x2*)(lds + L_C + ((u * 16 + tok) * 36 + cc) * 4);
    float b[2] = {bflo(xv), bfhi(xv)};
#pragma unroll
    for (int r = 0; r < 8; ++r)
      if (*(LAS unsigned*)(lds + L_IDX + r * 4) == (unsigned)gtok) {
#pragma unroll
        for (int i = 0; i < 2; ++i) b[i] = rbf(b[i] + *(LAS float*)(lds + L_Y + (r * 32 + cc + i) * 4));
      }
    const float og = rbf(1.0f - gt);
    *(unsigned*)(p.out + (size_t)gtok * 512 + slice * 32 + cc) = cvtpk(rbf(gt * rbf(av[0])) + rbf(og * b[0]), rbf(gt * rbf(av[1])) + rbf(og * b[1]));
    BAR();
    STAMP((seg >> 1) * 8 + 2);
  }
  STAMP(1003);
  DBG_DUMP();
}

__global__ void __launch_bounds__(256, 1) rmt_mod_kernel(Params p) {
  extern __shared__ __attribute__((aligned(16))) unsigned char smem[];
  ldsp lds = (ldsp)smem;
  const rsrc_t fl = __builtin_amdgcn_make_buffer_rsrc(p.flags, 0, FL_BYTES, 0x00020000);
  const rsrc_t ws = __builtin_amdgcn_make_buffer_rsrc(p.data, 0, WS_END, 0x00020000);
  const int x = blockIdx.x & 7, y = blockIdx.x >> 3;
  {
    unsigned long long pc;
    asm volatile("s_getpc_b64 %0" : "=s"(pc));
    const unsigned o = min((unsigned)(y * 2048 + threadIdx.x * 16), (unsigned)(CODE_BYTES - 16));
    if (rfl(threadIdx.x) < 128) glds16s((const void*)(pc & ~255ull), o, rfl(156 * 1024 + (threadIdx.x >> 6) * 1024));
  }
  if (x < 4) role_h(p, lds, fl, ws, x, y);
  else if (y < 16) role_w(p, lds, fl, ws, x - 4, y);
  else role_t(p, lds, fl, ws, x - 4, y);
}

__global__ void __launch_bounds__(64) rmt_prep_kernel(unsigned char* flags, unsigned char* data) {
  const rsrc_t fl = __builtin_amdgcn_make_buffer_rsrc(flags, 0, FL_BYTES, 0x00020000);
  const rsrc_t ws = __builtin_amdgcn_make_buffer_rsrc(data, 0, WS_END, 0x00020000);
  const int lane = threadIdx.x, b = blockIdx.x;
  if ((b & 7) == 0 && b < 16) {
    const unsigned base = (unsigned)((b >> 3) * 2 * MB + lane * 4);
    unsigned gap = 0;
    for (int it = 0; it < 2; ++it) {
      st1(SENT, ws, base, 0); st1(SENT, ws, base + 8192, 0);
      vmwait<1>();
      const unsigned t1 = mtime();
      vmwait<0>();
      gap = mtime() - t1;
    }
    if (lane == 0) st1((gap > 130u ? 0u : 1u) | (gap << 8), fl, FL_POL + (b >> 3) * 4, 0);
  } else {
    const int i = b < 16 ? b - (b >> 3) - 1 : b - 2;
    const u32x4 z = {0u, 0u, 0u, 0u};
    const int o = i * 1024 + lane * 16;
    if (o != FL_POL && o < FL_BYTES) st4(z, fl, o, 0);
  }
}

#define TUNE_D1 8
#define TUNE_D2 56
extern "C" void kernel_launch(void* const* d_in, const int* in_sizes, int n_in, void* d_out, int out_size, void* d_ws, size_t ws_size, hipStream_t stream) {
  (void)in_sizes; (void)n_in; (void)out_size; (void)ws_size;
  Params p;
  p.x = (const uint16_t*)d_in[0]; p.mem0 = (const uint16_t*)d_in[1];
  p.wq = (const uint16_t*)d_in[2]; p.wk = (const uint16_t*)d_in[3]; p.wv = (const uint16_t*)d_in[4]; p.wo = (const uint16_t*)d_in[5];
  p.wr = (const uint16_t*)d_in[8]; p.wg = (const uint16_t*)d_in[9]; p.wu = (const uint16_t*)d_in[10]; p.wd = (const uint16_t*)d_in[11];
  p.gw = (const uint16_t*)d_in[12];
  p.out = (uint16_t*)d_out;
  p.flags = (unsigned char*)d_ws;
  p.data = (unsigned char*)(((uintptr_t)d_ws + FL_BYTES + 2 * MB - 1) & ~(uintptr_t)(2 * MB - 1));
  p.d1 = TUNE_D1; p.d2 = TUNE_D2; p.pen = 30; p.t0 = 160; p.tdel = 9000; p.zb0 = 112; p.cap = 6; p.pad1 = 0;
  (void)hipFuncSetAttribute((const void*)rmt_mod_kernel, hipFuncAttributeMaxDynamicSharedMemorySize, LDS_BYTES);
  static_assert(FL_BYTES <= 27 * 1024, "prep grid");
  rmt_prep_kernel<<<29, 64, 0, stream>>>(p.flags, p.data);
  rmt_mod_kernel<<<256, 256, LDS_BYTES, stream>>>(p);
}
```
